```python
import torch

def _softmax(x, axis=-1):
    return torch.softmax(x, dim=axis)
def _gelu(x):
    return torch.nn.functional.gelu(x, approximate="tanh")
def _silu(x):
    return torch.nn.functional.silu(x)
def _sigmoid(x):
    return torch.sigmoid(x)
def _layernorm(x, axis=-1, eps=1e-5):
    m = x.mean(dim=axis, keepdim=True)
    v = x.var(dim=axis, keepdim=True, unbiased=False)
    return (x - m) / torch.sqrt(v + eps)
def _scatter_add(tgt, idx, src, axis=0):
    return tgt.clone().index_add_(axis, idx, src)
def _segment_sum(src, seg, n):
    out = torch.zeros((n,) + src.shape[1:], dtype=src.dtype)
    return out.index_add_(0, seg, src)
def _pad(x, pw):
    flat = tuple(v for pair in reversed(pw) for v in pair)
    return torch.nn.functional.pad(x, flat)

kernel_name = 'concat_heads__sliding_local_dilated__linear_attn_block__prompt'

def setup_inputs():

    _w = (lambda a: (lambda t: t.to(torch.bfloat16) if t.is_floating_point() else t)(torch.as_tensor(a)))
    def _A_setup():
        B, S, D = 1, 2048, 512
        return {
            "x": _w((lambda *s: torch.randn(*s, dtype=torch.bfloat16) * 0.2)(B, S, D)),
            "wqkv": _w((lambda *s: torch.randn(*s, dtype=torch.bfloat16) * 0.2)(D, 3 * D)),
            "wo": _w((lambda *s: torch.randn(*s, dtype=torch.bfloat16) * 0.05)(D, D)),
        }
    _A_in = _A_setup()

    def _B_setup():
        B, S, D = 1, 2048, 512
        return {
            "x": _w((lambda *s: torch.randn(*s, dtype=torch.bfloat16) * 0.05)(B, S, D)),
            "wqkv": _w((lambda *s: torch.randn(*s, dtype=torch.bfloat16) * 1.6)(D, 3 * D)),
            "wo": _w((lambda *s: torch.randn(*s, dtype=torch.bfloat16) * 0.8)(D, D)),
        }
    _B_in = _B_setup()

    return {
        "A_x": _A_in["x"],
        "A_wqkv": _A_in["wqkv"],
        "A_wo": _A_in["wo"],
        "B_x": _B_in["x"],
        "B_wqkv": _B_in["wqkv"],
        "B_wo": _B_in["wo"],
    }

def reference(A_x, A_wqkv, A_wo, B_x, B_wqkv, B_wo):
    def _A_ref(x, wqkv, wo):
        B, S, D = x.shape
        H = 32; Dh = D // H
        W, dil = 8, 2
        qkv = (lambda x, *d: x.permute(*d))((x @ wqkv).reshape(B, S, 3, H, Dh), 2, 0, 3, 1, 4)
        q, k, v = qkv[0], qkv[1], qkv[2]
        scale = Dh ** -0.5
        pos = torch.arange(S)
        off = torch.arange(W) * dil
        idx = pos[:, None] - off[None, :]
        mask = idx >= 0
        idx_c = torch.where(mask, idx, 0).reshape(-1)
        kg = k[:, :, idx_c, :].reshape(B, H, S, W, Dh)
        vg = v[:, :, idx_c, :].reshape(B, H, S, W, Dh)
        logits = torch.einsum("bhsd,bhswd->bhsw", q, kg) * scale
        logits = torch.where(mask[None, None, :, :], logits, -1e9)
        attn = _softmax(logits, axis=-1)
        o = torch.einsum("bhsw,bhswd->bhsd", attn, vg)
        return (lambda x, *d: x.permute(*d))(o, 0, 2, 1, 3).reshape(B, S, D) @ wo

    def _B_ref(x, wqkv, wo):
        B, S, D = x.shape
        H = 32; Dh = D // H
        qkv = (lambda x, *d: x.permute(*d))((x @ wqkv).reshape(B, S, 3, H, Dh), 2, 0, 3, 1, 4)
        q, k, v = qkv[0], qkv[1], qkv[2]
        phi = lambda t: torch.where(t > 0, t + 1, torch.exp(t)) + 1e-6
        pq, pk = phi(q), phi(k)
        kv = torch.einsum("bhsd,bhse->bhde", pk, v)
        z = torch.einsum("bhsd->bhd", pk)
        num = torch.einsum("bhqd,bhde->bhqe", pq, kv)
        den = torch.einsum("bhqd,bhd->bhq", pq, z)[..., None] + 1e-6
        o = num / den
        return (lambda x, *d: x.permute(*d))(o, 0, 2, 1, 3).reshape(B, S, D) @ wo

    a = _A_ref(A_x, A_wqkv, A_wo)
    b = _B_ref(B_x, B_wqkv, B_wo)
    return torch.concatenate([a, b], axis=-1)
```

```cpp
#include <hip/hip_runtime.h>
#include <hip/hip_bf16.h>
typedef __hip_bfloat16 bf16;
__device__ __forceinline__ float b2f(bf16 x){ return __bfloat162float(x);}
__device__ __forceinline__ float rb(float x){ return __bfloat162float(__float2bfloat16(x)); }

__global__ void gemm_naive(const bf16* A, const bf16* B, bf16* C, int M, int N, int K, int ldc) {
  int n = blockIdx.x * 64 + (threadIdx.x & 63);
  int m = blockIdx.y * 4 + (threadIdx.x >> 6);
  float acc = 0.f;
  for (int k = 0; k < K; ++k) acc += b2f(A[m * K + k]) * b2f(B[k * N + n]);
  C[m * ldc + n] = __float2bfloat16(acc);
}
__global__ void attnA(const bf16* qkv, bf16* o) {
  int idx = blockIdx.x * 256 + threadIdx.x;
  int h = idx & 31, s = idx >> 5;
  float lg[8]; float m = -1e30f;
  for (int w = 0; w < 8; ++w) {
    int p = s - 2 * w; float a = 0.f;
    if (p >= 0) { for (int d = 0; d < 16; ++d) a += b2f(qkv[s * 1536 + h * 16 + d]) * b2f(qkv[p * 1536 + 512 + h * 16 + d]); a *= 0.25f; } else a = -1e9f;
    lg[w] = a; m = fmaxf(m, a);
  }
  float sum = 0.f;
  for (int w = 0; w < 8; ++w) { lg[w] = __expf(lg[w] - m); sum += lg[w]; }
  for (int d = 0; d < 16; ++d) {
    float a = 0.f;
    for (int w = 0; w < 8; ++w) { int p = s - 2 * w; if (p < 0) p = 0; a += lg[w] / sum * b2f(qkv[p * 1536 + 1024 + h * 16 + d]); }
    o[s * 512 + h * 16 + d] = __float2bfloat16(a);
  }
}
__device__ __forceinline__ float phi(float t){ return rb((t > 0.f ? t + 1.f : __expf(t)) + 1e-6f); }
__global__ void kvB(const bf16* qkv, float* kv, float* z) {
  int h = blockIdx.x; int d = threadIdx.x >> 4, e = threadIdx.x & 15;
  float a = 0.f, zz = 0.f;
  for (int s = 0; s < 2048; ++s) { float pk = phi(b2f(qkv[s * 1536 + 512 + h * 16 + d])); a += pk * b2f(qkv[s * 1536 + 1024 + h * 16 + e]); zz += pk; }
  kv[h * 256 + d * 16 + e] = rb(a); if (e == 0) z[h * 16 + d] = rb(zz);
}
__global__ void oB(const bf16* qkv, const float* kv, const float* z, bf16* o) {
  int idx = blockIdx.x * 256 + threadIdx.x;
  int e = idx & 15, h = (idx >> 4) & 31, s = idx >> 9;
  float num = 0.f, den = 0.f;
  for (int d = 0; d < 16; ++d) { float pq = phi(b2f(qkv[s * 1536 + h * 16 + d])); num += pq * kv[h * 256 + d * 16 + e]; den += pq * z[h * 16 + d]; }
  o[s * 512 + h * 16 + e] = __float2bfloat16(num / (den + 1e-6f));
}
extern "C" void kernel_launch(void* const* d_in, const int* in_sizes, int n_in, void* d_out, int out_size, void* d_ws, size_t ws_size, hipStream_t stream) {
  const bf16* Ax = (const bf16*)d_in[0]; const bf16* Aw = (const bf16*)d_in[1]; const bf16* Ao = (const bf16*)d_in[2];
  const bf16* Bx = (const bf16*)d_in[3]; const bf16* Bw = (const bf16*)d_in[4]; const bf16* Bo = (const bf16*)d_in[5];
  bf16* out = (bf16*)d_out;
  char* ws = (char*)d_ws;
  bf16* qa = (bf16*)ws; bf16* qb = (bf16*)(ws + (8 << 20)); bf16* oa = (bf16*)(ws + (16 << 20)); bf16* ob = (bf16*)(ws + (20 << 20));
  float* kv = (float*)(ws + (24 << 20)); float* z = (float*)(ws + (25 << 20));
  gemm_naive<<<dim3(24, 512), 256, 0, stream>>>(Ax, Aw, qa, 2048, 1536, 512, 1536);
  gemm_naive<<<dim3(24, 512), 256, 0, stream>>>(Bx, Bw, qb, 2048, 1536, 512, 1536);
  attnA<<<256, 256, 0, stream>>>(qa, oa);
  kvB<<<32, 256, 0, stream>>>(qb, kv, z);
  oB<<<4096, 256, 0, stream>>>(qb, kv, z, ob);
  gemm_naive<<<dim3(8, 512), 256, 0, stream>>>(oa, Ao, out, 2048, 512, 512, 1024);
  gemm_naive<<<dim3(8, 512), 256, 0, stream>>>(ob, Bo, out + 512, 2048, 512, 512, 1024);
}
```

```cpp
#include <hip/hip_runtime.h>
#include <hip/hip_bf16.h>

typedef float f32x4 __attribute__((ext_vector_type(4)));
typedef float f32x2 __attribute__((ext_vector_type(2)));
typedef __bf16 b16x8 __attribute__((ext_vector_type(8)));
typedef __bf16 b16x2 __attribute__((ext_vector_type(2)));
typedef short s16x4 __attribute__((ext_vector_type(4)));
typedef short s16x8 __attribute__((ext_vector_type(8)));
typedef unsigned u32x4 __attribute__((ext_vector_type(4)));
typedef unsigned u32x2 __attribute__((ext_vector_type(2)));
typedef unsigned long long u64;
#define LAS __attribute__((address_space(3)))
#define GAS __attribute__((address_space(1)))
#define DI __device__ __forceinline__
#define RLX_AGENT __ATOMIC_RELAXED, __HIP_MEMORY_SCOPE_AGENT

constexpr int L_HALO = 0;
constexpr int L_X = 16384;
constexpr int L_W = 81920;
constexpr int L_SP = 155648;
constexpr int LDS_BYTES = 163840;
constexpr int L_STAGE = L_X + 3 * 16384;
constexpr int NWS = 6;

constexpr unsigned WS_O = 0;
constexpr unsigned WS_KV = 4u << 20;
constexpr unsigned WS_Z = WS_KV + (512u << 10);
constexpr unsigned WS_FO = WS_Z + (32u << 10);
constexpr unsigned WS_FK = WS_FO + (32u << 10);
constexpr unsigned WS_DIAG = WS_FK + (32u << 10);
constexpr unsigned WS_USED = WS_DIAG + (1u << 20);
constexpr u64 TAG_STEP = 0x9E3779B97F4A7C15ull;
constexpr unsigned OOB = 0x80000000u;
constexpr int SPIN_CAP = 1 << 18;

constexpr int wo_unit_addr(int u) {
  if (u < 2) return L_SP + u * 4096;
  if (u == 2) return L_HALO;
  if (u == 3) return L_HALO + 4096;
  if (u < 8) return L_X + (u - 4) * 4096;
  if (u < 11) return L_W + 4 * 12288 + (u - 8) * 4096;
  if (u < 14) return L_W + 5 * 12288 + (u - 11) * 4096;
  if (u < 18) return L_X + 16384 + (u - 14) * 4096;
  if (u == 18) return L_HALO + 8192;
  if (u < 22) return L_W + (u - 19) * 4096;
  if (u < 25) return L_W + 12288 + (u - 22) * 4096;
  if (u < 29) return L_X + 2 * 16384 + (u - 25) * 4096;
  return L_W + 2 * 12288 + (u - 29) * 4096;
}
constexpr int wo_beg(int s) {
  return s <= 3 ? 2 : s <= 7 ? 3 : s <= 9 ? 4 : s == 10 ? 8 : s == 11 ? 11 : s == 12 ? 19 : s == 13 ? 22 : s == 14 ? 29 : 32;
}
constexpr int rel_cnt(int s) {
  return (s + NWS <= 15 ? 3 : 0) + (((s & 1) && s <= 7) ? 4 : 0) + (wo_beg(s + 1) - wo_beg(s));
}
constexpr int PRO_CNT = 40;
constexpr int issued_before(int s) {
  int n = PRO_CNT;
  for (int i = 0; i + 2 <= s; ++i) n += rel_cnt(i);
  return n;
}
constexpr int pos_w(int s) {
  if (s < NWS) { const int p[6] = {8, 11, 18, 21, 29, 32}; return p[s]; }
  int r = s - NWS;
  return issued_before(r + 1) + ((((r & 1) && r <= 7)) ? 4 : 0) + 3;
}
constexpr int pos_x(int k) {
  if (k < 4) { const int p[4] = {5, 15, 26, 36}; return p[k]; }
  int r = 2 * (k - 4) + 1;
  return issued_before(r + 1) + 4;
}
constexpr int pos_h(int p) { const int q[4] = {1, 22, 37, 38}; return q[p]; }
constexpr int imax(int a, int b) { return a > b ? a : b; }
constexpr int wait_n(int s) { return issued_before(s) - imax(pos_w(s), imax(pos_x(s >> 1), pos_h(s >> 2))); }

struct Params {
  const char* x[2];
  const char* w[2];
  const char* wo[2];
  char* out;
  char* ws;
};

DI __amdgpu_buffer_rsrc_t mkrsrc(const void* p, unsigned bytes) {
  return __builtin_amdgcn_make_buffer_rsrc((void*)p, 0, (int)bytes, 0x00020000);
}
#define DMA(rs, ldsoff, voff, soff, imm) \
  __builtin_amdgcn_raw_ptr_buffer_load_lds((rs), (LAS void*)(lds + (ldsoff)), 16, (voff), (soff), (imm), 0)
#define MFMA32(a, b, c) __builtin_amdgcn_mfma_f32_16x16x32_bf16((a), (b), (c), 0, 0, 0)
#define MFMA16(a, b, c) __builtin_amdgcn_mfma_f32_16x16x16bf16_1k((a), (b), (c), 0, 0, 0)
#define SCHED0() __builtin_amdgcn_sched_barrier(0)

#define STAMP(i) do {} while (0)

DI unsigned pk2(float a, float b) {
  f32x2 f = {a, b};
  return __builtin_bit_cast(unsigned, __builtin_convertvector(f, b16x2));
}
DI s16x4 pk4(f32x4 v) {
  u32x2 u = {pk2(v[0], v[1]), pk2(v[2], v[3])};
  return __builtin_bit_cast(s16x4, u);
}
DI b16x8 cat8(s16x4 a, s16x4 b) {
  return __builtin_bit_cast(b16x8, __builtin_shufflevector(a, b, 0, 1, 2, 3, 4, 5, 6, 7));
}
DI s16x4 trread(LAS char* p) { return __builtin_amdgcn_ds_read_tr16_b64_v4i16((LAS s16x4*)p); }
DI float fmx(float a, float b) { return __builtin_elementwise_maximum(a, b); }
DI float phi(float t) {
  float e = __builtin_amdgcn_fmed3f(__builtin_amdgcn_exp2f(t * 1.44269504088896f), 0.f, 1.f);
  return (fmx(t, 0.f) + 1e-6f) + e;
}
DI float rbf(float t) { return __builtin_bit_cast(float, pk2(t, t) << 16); }
DI s16x4 phi4(f32x4 v) {
  f32x4 r;
  r[0] = phi(v[0]); r[1] = phi(v[1]); r[2] = phi(v[2]); r[3] = phi(v[3]);
  return pk4(r);
}
template <int CTRL> DI float dpp_add(float x) {
  return x + __builtin_bit_cast(float, __builtin_amdgcn_update_dpp(0, __builtin_bit_cast(int, x), CTRL, 0xf, 0xf, true));
}
DI float row16_sum(float x) {
  x = dpp_add<0xB1>(x);
  x = dpp_add<0x4E>(x);
  x = dpp_add<0x141>(x);
  x = dpp_add<0x140>(x);
  return x;
}
DI void wait_flags(const char* p, int n, u64 tag, int lane) {
  GAS u64* f = (GAS u64*)p + (lane & (n - 1));
  for (int spins = 0; spins < SPIN_CAP; ++spins) {
    u64 v = __hip_atomic_load(f, RLX_AGENT);
    if (__all(v == tag)) break;
    __builtin_amdgcn_s_sleep(1);
  }
  __builtin_amdgcn_fence(__ATOMIC_ACQUIRE, "wavefront");
  asm volatile("" ::: "memory");
}

template <int S> DI void loader_step(LAS char* lds, __amdgpu_buffer_rsrc_t rx, __amdgpu_buffer_rsrc_t rw,
                                     __amdgpu_buffer_rsrc_t ro, unsigned vx, unsigned vw, unsigned vo, int j, u64* ts) {
  if constexpr (S <= 15) asm volatile("s_waitcnt vmcnt(%0)\n\ts_barrier" ::"i"(wait_n(S)) : "memory");
  else asm volatile("s_barrier" ::: "memory");
  constexpr int R = S - 1;
  if constexpr (R >= 0 && R <= 14) {
    if constexpr ((R & 1) && R <= 7) {
      constexpr int K = (R - 1) / 2 + 4;
      const int lo = L_X + (K & 3) * 16384 + j * 4096;
      DMA(rx, lo, vx, K * 128, 0);
      DMA(rx, lo, vx, K * 128 + 7168, 1024);
      DMA(rx, lo, vx, K * 128 + 2 * 7168, 2048);
      DMA(rx, lo, vx, K * 128 + 3 * 7168, 3072);
    }
    if constexpr (R + NWS <= 15) {
      constexpr int T = R + NWS;
      const int lo = L_W + (T % NWS) * 12288 + j * 3072;
      DMA(rw, lo, vw, T * 98304, 0);
      DMA(rw, lo, vw, T * 98304, 1024);
      DMA(rw, lo, vw, T * 98304, 2048);
    }
#pragma unroll
    for (int u = wo_beg(R); u < wo_beg(R + 1); ++u) DMA(ro, wo_unit_addr(u) + j * 1024, vo, u * 16384, 0);
  }
  if constexpr (S < 16) loader_step<S + 1>(lds, rx, rw, ro, vx, vw, vo, j, ts);
}

template <bool IS_A> struct Acc {
  f32x4 q[8], k[9], v[9];
};
struct Frag {
  b16x8 x[9];
  b16x8 w[3];
};

template <bool IS_A, int S> DI void read_frags(Frag& f, LAS char* xb, LAS char* wb) {
  constexpr int K = S >> 1;
  const int ks = (S & 1) * 64;
  LAS char* xp = (LAS char*)((unsigned)(size_t)xb ^ ks);
  LAS char* wp = wb + L_W + (S % NWS) * 12288;
#pragma unroll
  for (int sg = 0; sg < 3; ++sg) f.w[sg] = cat8(trread(wp + sg * 1024), trread(wp + sg * 1024 + 512));
  if constexpr (IS_A) f.x[0] = *(LAS b16x8*)(xp + L_HALO + K * 2048);
#pragma unroll
  for (int b = 0; b < 8; ++b) f.x[b + 1] = *(LAS b16x8*)(xp + L_X + (K & 3) * 16384 + b * 2048);
}
template <bool IS_A> DI void mma_step(Acc<IS_A>& a, const Frag& f) {
  if constexpr (IS_A) {
    a.k[0] = MFMA32(f.w[1], f.x[0], a.k[0]);
    a.v[0] = MFMA32(f.x[0], f.w[2], a.v[0]);
  }
#pragma unroll
  for (int b = 0; b < 8; ++b) {
    a.q[b] = MFMA32(f.w[0], f.x[b + 1], a.q[b]);
    if constexpr (IS_A) a.k[b + 1] = MFMA32(f.w[1], f.x[b + 1], a.k[b + 1]);
    else a.k[b + 1] = MFMA32(f.x[b + 1], f.w[1], a.k[b + 1]);
    a.v[b + 1] = MFMA32(f.x[b + 1], f.w[2], a.v[b + 1]);
  }
}
template <bool IS_A, int S> DI void gemm1_step(Acc<IS_A>& a, Frag& f0, Frag& f1, LAS char* xb, LAS char* wb) {
  SCHED0();
  asm volatile("s_waitcnt lgkmcnt(0)\n\ts_barrier" ::: "memory");
  SCHED0();
  if constexpr (S <= 15) read_frags<IS_A, S>((S & 1) ? f1 : f0, xb, wb);
  if constexpr (S >= 1) mma_step<IS_A>(a, (S & 1) ? f0 : f1);
  if constexpr (S >= 1 && S <= 15) {
#pragma unroll
    for (int i = 0; i < (IS_A ? 15 : 14); ++i) {
      __builtin_amdgcn_sched_group_barrier(0x8, 1, 0);
      __builtin_amdgcn_sched_group_barrier(0x100, 1, 0);
    }
    __builtin_amdgcn_sched_group_barrier(0x8, 16, 0);
  }
  if constexpr (S < 16) gemm1_step<IS_A, S + 1>(a, f0, f1, xb, wb);
}

template <bool IS_A>
DI void compute_path(const Params& P, LAS char* lds, int wave, int lane, int r, int g) {
  constexpr int BR = IS_A ? 0 : 1;
  const int l15 = lane & 15, lg = lane >> 4;
  const char* fo = P.ws + WS_FO + ((BR * 16 + r) * 4 + wave) * 256;
  const int head = g * 4 + wave;
  const char* fk = P.ws + WS_FK + head * 256;
  const u64 tag_o = __hip_atomic_load((GAS u64*)(fo + 128), RLX_AGENT) + TAG_STEP;
  u64 tag_k = 0;
  if constexpr (!IS_A) tag_k = __hip_atomic_load((GAS u64*)(fk + 128), RLX_AGENT) + TAG_STEP;
  const __amdgpu_buffer_rsrc_t rws = mkrsrc(P.ws, WS_USED);

  Acc<IS_A> a;
  const f32x4 zero = {0.f, 0.f, 0.f, 0.f};
#pragma unroll
  for (int b = 0; b < 8; ++b) a.q[b] = zero;
#pragma unroll
  for (int b = 0; b < 9; ++b) { a.k[b] = zero; a.v[b] = zero; }
  Frag f0, f1;
  {
    LAS char* xb = lds + l15 * 128 + ((lg ^ (l15 & 6)) * 16);
    const int q = l15 >> 2, p = l15 & 3;
    const int fsw = ((q >> 1) & 1) | ((lg & 1) << 1);
    LAS char* wb = lds + lg * 3072 + q * 128 + ((wave ^ fsw) * 32) + p * 8;
    gemm1_step<IS_A, 0>(a, f0, f1, xb, wb);
  }
  STAMP(1);
  const s16x8 ones_s = {0x3F80, 0x3F80, 0x3F80, 0x3F80, 0x3F80, 0x3F80, 0x3F80, 0x3F80};
  const b16x8 ones = __builtin_bit_cast(b16x8, ones_s);
  LAS char* stg = lds + L_STAGE + (wave >> 1) * 1024 + lane * 16 + (wave & 1) * 8;

  if constexpr (IS_A) {
    const float NEG = -__builtin_inff();
    f32x4 cprev, ccur;
    unsigned msk[4];
#pragma unroll
    for (int i = 0; i < 4; ++i) {
      const int c = 4 * lg + i;
      const bool par = ((c ^ l15) & 1) == 0;
      cprev[i] = (par && c > l15) ? 0.f : NEG;
      ccur[i] = (par && c <= l15) ? 0.f : NEG;
    }
    const unsigned half = (l15 & 1) ? 0xFFFF0000u : 0x0000FFFFu;
    const int cA = 4 * lg + (l15 & 1), cB = cA + 2;
    msk[0] = cA > l15 ? half : 0u;
    msk[1] = cB > l15 ? half : 0u;
    msk[2] = cA > l15 ? 0u : half;
    msk[3] = cB > l15 ? 0u : half;
    f32x4 cfirst = cprev;
    if (r == 0) cfirst = (f32x4){NEG, NEG, NEG, NEG};
    s16x4 kf[9], vf[9];
#pragma unroll
    for (int b = 0; b < 9; ++b) { kf[b] = pk4(a.k[b]); vf[b] = pk4(a.v[b]); }
    const float C1 = 0.25f * 1.44269504088896f;
#pragma unroll
    for (int b = 0; b < 8; ++b) {
      const s16x4 qf = pk4(a.q[b]);
      const f32x4 s0 = MFMA16(kf[b], qf, b == 0 ? cfirst : cprev);
      const f32x4 s1 = MFMA16(kf[b + 1], qf, ccur);
      const float t0 = fmx(fmx(s0[0], s0[1]), fmx(s1[0], s1[1]));
      const float t1 = fmx(fmx(s0[2], s0[3]), fmx(s1[2], s1[3]));
      float m = fmx(t0, t1);
      {
        auto u = __builtin_amdgcn_permlane16_swap(__float_as_uint(m), __float_as_uint(m), false, false);
        m = fmx(__uint_as_float(u[0]), __uint_as_float(u[1]));
        auto v = __builtin_amdgcn_permlane32_swap(__float_as_uint(m), __float_as_uint(m), false, false);
        m = fmx(__uint_as_float(v[0]), __uint_as_float(v[1]));
      }
      const float mc = m * C1;
      const float e0 = __builtin_amdgcn_exp2f(__builtin_fmaf(t0, C1, -mc));
      const float e1 = __builtin_amdgcn_exp2f(__builtin_fmaf(t1, C1, -mc));
      const unsigned p0 = pk2(e0, e0), p1 = pk2(e1, e1);
      const u32x4 pu = {p0 & msk[0], p1 & msk[1], p0 & msk[2], p1 & msk[3]};
      const b16x8 pf = __builtin_bit_cast(b16x8, pu);
      const f32x4 o = MFMA32(cat8(vf[b], vf[b + 1]), pf, zero);
      const f32x4 sm = MFMA32(ones, pf, zero);
      const float inv = __builtin_amdgcn_rcpf(sm[0]);
      *(LAS s16x4*)(stg + b * 2048) = pk4(o * inv);
    }
  } else {
    f32x4 kv = zero, zz = zero;
#pragma unroll
    for (int i = 0; i < 4; ++i) {
      const b16x8 pkf = cat8(phi4(a.k[2 * i + 1]), phi4(a.k[2 * i + 2]));
      const b16x8 vf = cat8(pk4(a.v[2 * i + 1]), pk4(a.v[2 * i + 2]));
      kv = MFMA32(pkf, vf, kv);
      zz = MFMA32(pkf, ones, zz);
    }
    __builtin_amdgcn_raw_buffer_store_b128(__builtin_bit_cast(u32x4, kv), rws, WS_KV + (head * 16 + r) * 1024 + lane * 16, 0, 16);
    __builtin_amdgcn_raw_buffer_store_b128(__builtin_bit_cast(u32x4, zz), rws,
                                           l15 == 0 ? WS_Z + (head * 16 + r) * 64 + lg * 16 : OOB, 0, 16);
    asm volatile("s_waitcnt vmcnt(0)" ::: "memory");
    if (lane == 0) __hip_atomic_store((GAS u64*)fk + r, tag_k, RLX_AGENT);
    STAMP(2);
    s16x4 pq[8];
#pragma unroll
    for (int b = 0; b < 8; ++b) pq[b] = phi4(a.q[b]);
    wait_flags(fk, 16, tag_k, lane);
    STAMP(3);
    if (r == 0 && lane == 0) __hip_atomic_store((GAS u64*)(fk + 128), tag_k, RLX_AGENT);
    f32x4 part[16];
#pragma unroll
    for (int i = 0; i < 16; ++i)
      part[i] = __builtin_bit_cast(f32x4, __builtin_amdgcn_raw_buffer_load_b128(rws, WS_KV + head * 16384 + lane * 16, i * 1024, 16));
    f32x4 zp = __builtin_bit_cast(f32x4, __builtin_amdgcn_raw_buffer_load_b128(rws, WS_Z + head * 1024 + l15 * 64 + lg * 16, 0, 16));
#pragma unroll
    for (int st = 1; st < 16; st <<= 1)
#pragma unroll
      for (int i = 0; i < 16; i += 2 * st) part[i] += part[i + st];
#pragma unroll
    for (int i = 0; i < 4; ++i) zp[i] = row16_sum(zp[i]);
    const s16x4 kvb = pk4(part[0]), zb = pk4(zp);
    const f32x4 eps = {1e-6f, 1e-6f, 1e-6f, 1e-6f};
#pragma unroll
    for (int b = 0; b < 8; ++b) {
      const f32x4 num = MFMA16(kvb, pq[b], zero);
      const f32x4 den = MFMA16(zb, pq[b], eps);
      const float inv = __builtin_amdgcn_rcpf(den[0]);
      *(LAS s16x4*)(stg + b * 2048) = pk4(num * inv);
    }
  }
  STAMP(4);
  asm volatile("s_waitcnt lgkmcnt(0)\n\ts_barrier" ::: "memory");
  {
    const unsigned ob = WS_O + (BR * 16 + r) * 131072 + g * 2048 + lane * 16;
#pragma unroll
    for (int i = 0; i < 2; ++i)
#pragma unroll
      for (int t = 0; t < 2; ++t) {
        const int b = wave + 4 * i;
        const u32x4 v = *(LAS u32x4*)(lds + L_STAGE + b * 2048 + t * 1024 + lane * 16);
        __builtin_amdgcn_raw_buffer_store_b128(v, rws, ob + b * 16384 + t * 1024, 0, 16);
      }
    asm volatile("s_waitcnt vmcnt(0)" ::: "memory");
    if (lane == 0) __hip_atomic_store((GAS u64*)fo + g, tag_o, RLX_AGENT);
  }
  STAMP(5);
  const int blk = (g >> 2) * 4 + wave;
  wait_flags(fo, 8, tag_o, lane);
  STAMP(6);
  if (g == 0 && lane == 0) __hip_atomic_store((GAS u64*)(fo + 128), tag_o, RLX_AGENT);
  b16x8 of[16];
  {
    const unsigned ob = WS_O + (BR * 16 + r) * 131072 + blk * 16384 + lane * 16;
#pragma unroll
    for (int t = 0; t < 16; ++t) of[t] = __builtin_bit_cast(b16x8, __builtin_amdgcn_raw_buffer_load_b128(rws, ob, t * 1024, 16));
  }
  f32x4 c2[8];
#pragma unroll
  for (int j = 0; j < 8; ++j) c2[j] = zero;
  {
    const int q = l15 >> 2, p = l15 & 3;
    const int sw = q | ((lg & 1) << 2);
    LAS char* ub = lds + lg * 1024 + q * 256 + p * 8;
#pragma unroll
    for (int t = 0; t < 16; ++t) {
#pragma unroll
      for (int j = 0; j < 8; ++j) {
        LAS char* pj = ub + ((j ^ sw) * 32);
        const b16x8 wf = cat8(trread(pj + wo_unit_addr(2 * t)), trread(pj + wo_unit_addr(2 * t + 1)));
        c2[j] = MFMA32(wf, of[t], c2[j]);
      }
    }
  }
  STAMP(7);
  {
    const __amdgpu_buffer_rsrc_t rout = mkrsrc(P.out, 2048 * 2048);
    const unsigned row = r * 128 + blk * 16 + l15;
    const unsigned ob = row * 2048 + BR * 1024 + (g & 3) * 256 + ((lg & 1) * 32 + (lg >> 1) * 16);
#pragma unroll
    for (int j = 0; j < 8; j += 2) {
      const u32x2 lo = __builtin_bit_cast(u32x2, pk4(c2[j])), hi = __builtin_bit_cast(u32x2, pk4(c2[j + 1]));
      auto s0 = __builtin_amdgcn_permlane16_swap(lo[0], hi[0], false, false);
      auto s1 = __builtin_amdgcn_permlane16_swap(lo[1], hi[1], false, false);
      const u32x4 v = {s0[0], s1[0], s0[1], s1[1]};
      __builtin_amdgcn_raw_buffer_store_b128(v, rout, ob + j * 32, 0, 0);
    }
  }
  STAMP(8);
}

__global__ __launch_bounds__(512, 2) void fused_kernel(Params P) {
  extern __shared__ __attribute__((aligned(16))) char lds_raw[];
  LAS char* lds = (LAS char*)lds_raw;
  const int wave = __builtin_amdgcn_readfirstlane(threadIdx.x >> 6);
  const int lane = threadIdx.x & 63;
  const int bid = blockIdx.x;
  const int xcd = bid & 7, idx = bid >> 3;
  const int br = xcd >> 2;
  const int r = (xcd & 3) * 4 + (idx >> 3), g = idx & 7;
  STAMP(0);
  if (wave >= 4) {
    const int j = wave - 4;
    const __amdgpu_buffer_rsrc_t rx = mkrsrc(P.x[br], 2048 * 1024);
    const __amdgpu_buffer_rsrc_t rw = mkrsrc(P.w[br], 512 * 3072);
    const __amdgpu_buffer_rsrc_t ro = mkrsrc(P.wo[br], 512 * 1024);
    const int row8 = lane >> 3, ch = lane & 7;
    const unsigned xsw = row8 * 1024 + ((ch ^ (row8 & 6)) * 16);
    const unsigned vx = (r * 128 + j * 32) * 1024 + xsw;
    const unsigned vh = (br == 0 && r > 0) ? (unsigned)((r * 128 - 16 + (j & 1) * 8) * 1024 + (j >> 1) * 128) + xsw : OOB;
    const int fsw = ((row8 >> 1) & 1) | ((j & 1) << 1);
    const unsigned vw = (j * 8 + row8) * 3072 + g * 128 + (((ch >> 1) ^ fsw) * 32) + (ch & 1) * 16;
    const int row4 = lane >> 4, c16 = lane & 15;
    const unsigned vo = (j * 4 + row4) * 1024 + (g & 3) * 256 + (((c16 >> 1) ^ (row4 | ((j & 1) << 2))) * 32) + (c16 & 1) * 16;
#define PH(p) DMA(rx, L_HALO + (p) * 4096 + j * 1024, vh, (p) * 256, 0)
#define PX(K) do { const int lo = L_X + (K) * 16384 + j * 4096; DMA(rx, lo, vx, (K) * 128, 0); DMA(rx, lo, vx, (K) * 128 + 7168, 1024); \
    DMA(rx, lo, vx, (K) * 128 + 2 * 7168, 2048); DMA(rx, lo, vx, (K) * 128 + 3 * 7168, 3072); } while (0)
#define PW(T) do { const int lo = L_W + (T) * 12288 + j * 3072; DMA(rw, lo, vw, (T) * 98304, 0); DMA(rw, lo, vw, (T) * 98304, 1024); \
    DMA(rw, lo, vw, (T) * 98304, 2048); } while (0)
    PH(0); PX(0); PW(0); PW(1); PX(1); PW(2); PW(3); PH(1); PX(2); PW(4); PW(5); PX(3); PH(2); PH(3);
    DMA(ro, wo_unit_addr(0) + j * 1024, vo, 0, 0);
    DMA(ro, wo_unit_addr(1) + j * 1024, vo, 16384, 0);
    u64 ts[18];
    loader_step<0>(lds, rx, rw, ro, vx, vw, vo, j, ts);
    asm volatile("s_waitcnt vmcnt(0)\n\ts_barrier" ::: "memory");
    STAMP(1);
    return;
  }
  if (br == 0) compute_path<true>(P, lds, wave, lane, r, g);
  else compute_path<false>(P, lds, wave, lane, r, g);
}

extern "C" void kernel_launch(void* const* d_in, const int* in_sizes, int n_in, void* d_out, int out_size, void* d_ws,
                              size_t ws_size, hipStream_t stream) {
  Params P;
  P.x[0] = (const char*)d_in[0];
  P.w[0] = (const char*)d_in[1];
  P.wo[0] = (const char*)d_in[2];
  P.x[1] = (const char*)d_in[3];
  P.w[1] = (const char*)d_in[4];
  P.wo[1] = (const char*)d_in[5];
  P.out = (char*)d_out;
  P.ws = (char*)d_ws;
  hipFuncSetAttribute((const void*)fused_kernel, hipFuncAttributeMaxDynamicSharedMemorySize, LDS_BYTES);
  hipLaunchKernelGGL(fused_kernel, dim3(256), dim3(512), LDS_BYTES, stream, P);
}
```

```cpp
#include <hip/hip_runtime.h>
#include <hip/hip_bf16.h>

typedef float f32x4 __attribute__((ext_vector_type(4)));
typedef float f32x2 __attribute__((ext_vector_type(2)));
typedef __bf16 b16x8 __attribute__((ext_vector_type(8)));
typedef __bf16 b16x2 __attribute__((ext_vector_type(2)));
typedef short s16x4 __attribute__((ext_vector_type(4)));
typedef short s16x8 __attribute__((ext_vector_type(8)));
typedef unsigned u32x4 __attribute__((ext_vector_type(4)));
typedef unsigned u32x2 __attribute__((ext_vector_type(2)));
typedef unsigned long long u64;
#define LAS __attribute__((address_space(3)))
#define GAS __attribute__((address_space(1)))
#define DI __device__ __forceinline__

#define PRO_BUDGET 21
#define STEP_BUDGET 6
#define EARLY_BUDGETS 0, 0, 0, 0
#define WQ_MIN 4
#define U_HEAD 4
#define U_MID 10
#define U_FIRST 10
#define U_REST 14
#define X_AUX 2
#define W_AUX 0
#define O_AUX 0
#define OUT_AUX 16
#define WO_PRE 3
#define BAR_MASK 0x15557
#define S_POST 10
#define S_CLR 4
#define S_CLRK 3
#define CLR_START 0
#define CLR_PER 1
#define LATE_RPG 2
#define CLR_STRIDE 1
#define O_DELAY 24
#define PUB_A 16
#define PUB_B 16
#define O_DELAY_B O_DELAY
#define WBL2_DELAY 16
#define PHI_EARLY 4
#define THROTTLE 8
#define ROT 1
#define MAP84 0

constexpr int L_X = 0;
constexpr int L_W = 65536;
constexpr int L_HALO = 139264;
constexpr int L_SP = 155648;
constexpr int LDS_BYTES = 163840;
constexpr int W_HI = 61440;
constexpr int QD = 8;
constexpr int REL_G = 16;

constexpr unsigned WS_O = 0;
constexpr unsigned WS_KV = 4u << 20;
constexpr unsigned WS_Z = WS_KV + (512u << 10);
constexpr unsigned WS_FO = WS_Z + (32u << 10);
constexpr unsigned WS_FK = WS_FO + (8u << 10);
constexpr unsigned WS_DIAG = WS_FK + (8u << 10);
constexpr unsigned WS_USED = WS_DIAG + (1u << 20);
constexpr u64 TAG_STEP = 0x9E3779B97F4A7C15ull;
constexpr unsigned OOB = 0x80000000u;
constexpr unsigned SENT = 0xFFFFFFFFu;
constexpr int SPIN_CAP = 1 << 18;

constexpr bool has_bar(int s) { return (BAR_MASK >> s) & 1; }
constexpr int bar_le(int s) { while (!has_bar(s)) --s; return s; }
constexpr int bar_gt(int s) { ++s; while (s <= 16 && !has_bar(s)) ++s; return s; }
static_assert(has_bar(0) && has_bar(16) && has_bar(S_POST) && S_CLRK < S_POST && has_bar(WQ_MIN) && has_bar(U_FIRST) && has_bar(U_REST), "barriers");
constexpr int clr_index(int s) {
  return (s >= S_CLR && (s - S_CLR) % CLR_STRIDE == 0 && (s - S_CLR) / CLR_STRIDE < 4 / CLR_PER) ? (s - S_CLR) / CLR_STRIDE : -1;
}
constexpr int clr_before(int s) { int n = 0; for (int i = 0; i < s; ++i) n += clr_index(i) >= 0 ? CLR_PER : 0; return n; }
static_assert(clr_before(16) == 4, "sentinel stores");
constexpr int w_addr(bool is_a, int s, int seg) {
  return (!is_a && seg == 0 && (s == QD || s == QD + 1)) ? L_HALO + (s - QD) * 4096 : L_W + ((s + 2) % 6) * 12288 + seg * 4096;
}

enum { K_H, K_X, K_W, K_U };
constexpr int MAXIT = 120;
struct Sched {
  int n;
  int kind[MAXIT], idx[MAXIT], sub[MAXIT], need[MAXIT], rel[MAXIT], iv[MAXIT];
  int beg[20];
  int wait[17];
  int uaddr[32];
  constexpr void add(int k, int i, int s, int nd, int rl) { kind[n] = k; idx[n] = i; sub[n] = s; need[n] = nd; rel[n] = rl; ++n; }
  constexpr void swap(int a, int b) {
    int* f[6] = {kind, idx, sub, need, rel, iv};
    for (int i = 0; i < 6; ++i) { const int t = f[i][a]; f[i][a] = f[i][b]; f[i][b] = t; }
  }
};
constexpr Sched make_sched(bool is_a) {
  Sched sc{};
  for (int s = 0; s < 16; s += 2) {
    const bool xlast = s == 2 && !has_bar(3) && !MAP84;
    if (is_a && (s & 3) == 0) sc.add(K_H, s >> 2, 0, s, -1);
    for (int p = 0; p < 3; ++p) {
      if (p == (xlast ? 2 : 0)) {
        for (int i = 0; i < 4; ++i) sc.add(K_X, s >> 1, i, s, s < 8 ? -1 : s - 7);
      } else {
        const int t = s + (xlast ? p : p - 1);
        for (int i = (!is_a && t >= QD) ? 1 : 0; i < 3; ++i) sc.add(K_W, t, i, t, t < 6 ? -1 : t - 6);
      }
    }
  }
  if (!is_a)
    for (int s = QD; s < 16; ++s) sc.add(K_W, s, 0, 16, s < QD + 2 ? -1 : s < QD + 6 ? s - 6 : s - 12);
  int nu = 0;
  for (int rl = -1; rl <= REL_G; ++rl) {
    int cand[40] = {}, nc = 0;
    for (int i = 0; i < 4; ++i)
      if (rl == (is_a ? 9 + 2 * i : REL_G))
        for (int u = 0; u < 4; ++u) cand[nc++] = L_X + i * 16384 + u * 4096;
    if (rl >= 10 && rl <= 15)
      for (int seg = is_a ? 0 : 1; seg < 3; ++seg)
        if (!is_a || w_addr(is_a, rl, seg) < 131072) cand[nc++] = w_addr(is_a, rl, seg);
    if (!is_a && rl == -1) { cand[nc++] = L_HALO + 8192; cand[nc++] = L_HALO + 12288; }
    if (!is_a && rl == S_POST - 1) { cand[nc++] = L_SP; cand[nc++] = L_SP + 4096; }
    for (int i = 0; i < nc && nu < 32; ++i) { sc.uaddr[nu] = cand[i]; sc.add(K_U, nu, 0, 99, rl); ++nu; }
  }
  int cur = -1, used = 0;
  for (int i = 0; i < sc.n; ++i) {
    const int av = sc.rel[i] < 0 ? -1 : bar_gt(sc.rel[i]);
    if (sc.need[i] >= 16) {
      const int lo = sc.kind[i] == K_W ? WQ_MIN : sc.idx[i] < U_HEAD ? U_FIRST : sc.idx[i] < U_HEAD + U_MID ? U_REST : 16;
      sc.iv[i] = av > lo ? av : lo;
      continue;
    }
    for (;;) {
      const int early[4] = {EARLY_BUDGETS};
      const int bud = cur < 0 ? PRO_BUDGET : (cur < 4 && early[cur] > 0) ? early[cur] : (bar_gt(cur) - cur) * STEP_BUDGET;
      if (av <= cur && used < bud) break;
      cur = bar_gt(cur);
      used = 0;
    }
    sc.iv[i] = cur;
    ++used;
  }
  for (int i = 1; i < sc.n; ++i)
    for (int k = i; k > 0 && sc.iv[k - 1] > sc.iv[k]; --k) sc.swap(k - 1, k);
  for (int s = -1; s <= 18; ++s) {
    int b = 0;
    while (b < sc.n && sc.iv[b] < s) ++b;
    sc.beg[s + 1] = b;
  }
  for (int s = 0; s <= 16; ++s) {
    int last = 0;
    for (int i = 0; i < sc.n; ++i)
      if (sc.need[i] <= 16 && bar_le(sc.need[i]) <= s) last = i + 1;
    sc.wait[s] = sc.beg[s + 1] - last;
  }
  return sc;
}
template <bool IS_A> struct SchedOf { static constexpr Sched v = make_sched(IS_A); };
constexpr bool sched_ok(const Sched& sc) {
  for (int s = 0; s <= 16; ++s)
    if (has_bar(s) && (sc.wait[s] < 0 || sc.wait[s] > 63)) return false;
  int nu = 0;
  for (int i = 0; i < sc.n; ++i) nu += sc.kind[i] == K_U;
  return nu == 32 && sc.n <= MAXIT;
}
static_assert(sched_ok(SchedOf<true>::v) && sched_ok(SchedOf<false>::v), "schedule");

struct Params {
  char* ws;
  char* out;
};

DI __amdgpu_buffer_rsrc_t mkrsrc(const void* p, unsigned bytes) {
  return __builtin_amdgcn_make_buffer_rsrc((void*)p, 0, (int)bytes, 0x00020000);
}
DI u32x4 mkrsrc4(const void* p, unsigned bytes) {
  const u64 a = (u64)p;
  return (u32x4){(unsigned)a, (unsigned)(a >> 32) & 0xFFFFu, bytes, 0x00020000u};
}
#define DMA DMA_
#define DMA_(rs, ldsoff, voff, soff, imm, aux) \
  __builtin_amdgcn_raw_ptr_buffer_load_lds((rs), (LAS void*)(lds + (ldsoff)), 16, (voff), (soff), (imm), (aux))
#define MFMA32(a, b, c) __builtin_amdgcn_mfma_f32_16x16x32_bf16((a), (b), (c), 0, 0, 0)
#define MFMA16(a, b, c) __builtin_amdgcn_mfma_f32_16x16x16bf16_1k((a), (b), (c), 0, 0, 0)
#define SCHED0() __builtin_amdgcn_sched_barrier(0)
#define SGB(mask, n) __builtin_amdgcn_sched_group_barrier((mask), (n), 0)
#define BARRIER() do { SCHED0(); asm volatile("s_barrier" ::: "memory"); SCHED0(); } while (0)

#define STAMP(i) do {} while (0)

DI int fresh_lane() {
  int l;
  asm volatile("v_mbcnt_lo_u32_b32 %0, -1, 0\n\tv_mbcnt_hi_u32_b32 %0, -1, %0" : "=v"(l));
  return l;
}
DI unsigned pk2(float a, float b) {
  f32x2 f = {a, b};
  return __builtin_bit_cast(unsigned, __builtin_convertvector(f, b16x2));
}
DI s16x4 pk4(f32x4 v) {
  u32x2 u = {pk2(v[0], v[1]), pk2(v[2], v[3])};
  return __builtin_bit_cast(s16x4, u);
}
DI b16x8 cat8(s16x4 a, s16x4 b) {
  return __builtin_bit_cast(b16x8, __builtin_shufflevector(a, b, 0, 1, 2, 3, 4, 5, 6, 7));
}
DI s16x4 trread(unsigned a) { return __builtin_amdgcn_ds_read_tr16_b64_v4i16((LAS s16x4*)(size_t)a); }
DI b16x8 ldsread(unsigned a) { return *(LAS b16x8*)(size_t)a; }
DI float fmx(float a, float b) { return __builtin_elementwise_maximum(a, b); }
constexpr float LOG2E = 1.44269504088896f;
constexpr float EPS = 1e-6f;
DI float phi(float u) {
  const float e = __builtin_amdgcn_fmed3f(__builtin_amdgcn_exp2f(__builtin_fmaf(u, LOG2E, -EPS * LOG2E)), 0.f, 1.f);
  return fmx(u, EPS) + e;
}
DI s16x4 phi4(f32x4 v) {
  f32x4 r;
  r[0] = phi(v[0]); r[1] = phi(v[1]); r[2] = phi(v[2]); r[3] = phi(v[3]);
  return pk4(r);
}
template <int CTRL> DI float dpp_add(float x) {
  return x + __builtin_bit_cast(float, __builtin_amdgcn_update_dpp(0, __builtin_bit_cast(int, x), CTRL, 0xf, 0xf, true));
}
DI float row16_sum(float x) {
  x = dpp_add<0xB1>(x);
  x = dpp_add<0x4E>(x);
  x = dpp_add<0x141>(x);
  x = dpp_add<0x140>(x);
  return x;
}
DI f32x2 swap16(float a, float b) {
  auto u = __builtin_amdgcn_permlane16_swap(__float_as_uint(a), __float_as_uint(b), false, false);
  return (f32x2){__uint_as_float(u[0]), __uint_as_float(u[1])};
}
DI f32x2 swap32(float a, float b) {
  auto u = __builtin_amdgcn_permlane32_swap(__float_as_uint(a), __float_as_uint(b), false, false);
  return (f32x2){__uint_as_float(u[0]), __uint_as_float(u[1])};
}
DI u32x4 pair_granule(s16x4 a, s16x4 b) {
  const u32x2 lo = __builtin_bit_cast(u32x2, a), hi = __builtin_bit_cast(u32x2, b);
  auto s0 = __builtin_amdgcn_permlane16_swap(lo[0], hi[0], false, false);
  auto s1 = __builtin_amdgcn_permlane16_swap(lo[1], hi[1], false, false);
  return (u32x4){s0[0], s1[0], s0[1], s1[1]};
}
DI u32x4 no_sent(u32x4 v) {
  v[0] = v[0] < SENT - 1 ? v[0] : SENT - 1;
  return v;
}
#define G_OPS(c, v) c "{v[192:195]}"(v[0]), c "{v[196:199]}"(v[1]), c "{v[200:203]}"(v[2]), c "{v[204:207]}"(v[3]), G_OPS1(c, v)
#define G_OPS1(c, v) c "{v[208:211]}"(v[4]), c "{v[212:215]}"(v[5]), c "{v[216:219]}"(v[6]), c "{v[220:223]}"(v[7]), G_OPS2(c, v)
#define G_OPS2(c, v) c "{v[224:227]}"(v[8]), c "{v[228:231]}"(v[9]), c "{v[232:235]}"(v[10]), c "{v[236:239]}"(v[11]), G_OPS3(c, v)
#define G_OPS3(c, v) c "{v[240:243]}"(v[12]), c "{v[244:247]}"(v[13]), c "{v[248:251]}"(v[14]), c "{v[252:255]}"(v[15])
#define G_LD(i)                                                                         \
  "s_add_i32 %[so], %[rot], " #i "*1024\n\ts_and_b32 %[so], %[so], 0x3c00\n\t"          \
  "buffer_load_dwordx4 v[192+4*" #i ":195+4*" #i "], %[vo], %[rs], %[so] offen sc1\n\t"
#define G_RE(i) "v_cmp_eq_u32 vcc, -1, v[192+4*" #i "]\n\ts_mov_b64 exec, vcc\n\t" G_LD(i) "s_mov_b64 exec, -1\n\t"
#define G_RE3 G_RE(12) G_RE(13) G_RE(14) G_RE(15)
#define G_RE2 G_RE(8) G_RE(9) G_RE(10) G_RE(11) G_RE3
#define G_RE1 G_RE(4) G_RE(5) G_RE(6) G_RE(7) G_RE2
#define G_RE0 G_RE(0) G_RE(1) G_RE(2) G_RE(3) G_RE1
#define G_MAX(i) "v_max3_u32 %[t], v[192+4*" #i "], v[196+4*" #i "], %[t]\n\t"
#define G_LOOP(W, CHECK, RELOAD)                                                                                        \
  "s_waitcnt vmcnt(" #W ")\n\ts_mov_b32 %[n], %[cap]\n.Lpoll%=:\n\t" CHECK                                              \
  "v_cmp_eq_u32 vcc, -1, %[t]\n\ts_cbranch_vccz .Ldone%=\n\ts_waitcnt vmcnt(0)\n\t" RELOAD                              \
  "s_waitcnt vmcnt(0)\n\ts_sub_u32 %[n], %[n], 1\n\ts_cbranch_scc0 .Lpoll%=\n.Ldone%=:"
#define G_TMP [t] "=&v"(t), [n] "=&s"(n), [so] "=&s"(so)
#define G_IN [vo] "v"(voff), [rs] "s"(rs), [rot] "s"(rot), [cap] "i"(SPIN_CAP)
DI void gather16(u32x4 (&v)[16], u32x4 rs, unsigned voff, int rot) {
  int so;
  asm volatile(G_LD(0) G_LD(1) G_LD(2) G_LD(3) G_LD(4) G_LD(5) G_LD(6) G_LD(7) G_LD(8) G_LD(9) G_LD(10) G_LD(11) G_LD(12) G_LD(13)
                   G_LD(14) G_LD(15)
               : G_OPS("=&", v), [so] "=&s"(so)
               : G_IN
               : "scc", "memory");
}
template <int C> DI void poll4(u32x4 (&v)[16], u32x4 rs, unsigned voff, int rot) {
  unsigned t;
  int n, so;
  if constexpr (C == 0)
    asm volatile(G_LOOP(12, "v_max_u32 %[t], v[192], v[196]\n\t" G_MAX(2), G_RE0) : G_OPS("+", v), G_TMP : G_IN : "vcc", "scc", "memory");
  if constexpr (C == 1)
    asm volatile(G_LOOP(8, "v_max_u32 %[t], v[208], v[212]\n\t" G_MAX(6), G_RE1) : G_OPS1("+", v), G_TMP : G_IN : "vcc", "scc", "memory");
  if constexpr (C == 2)
    asm volatile(G_LOOP(4, "v_max_u32 %[t], v[224], v[228]\n\t" G_MAX(10), G_RE2) : G_OPS2("+", v), G_TMP : G_IN : "vcc", "scc", "memory");
  if constexpr (C == 3)
    asm volatile(G_LOOP(0, "v_max_u32 %[t], v[240], v[244]\n\t" G_MAX(14), G_RE3) : G_OPS3("+", v), G_TMP : G_IN : "vcc", "scc", "memory");
}
DI void gather1(u32x4& z, u32x4 rs, unsigned voff) {
  asm volatile("buffer_load_dwordx4 v[188:191], %1, %2, 0 offen sc1" : "=&{v[188:191]}"(z) : "v"(voff), "s"(rs) : "memory");
}
DI void poll17(u32x4 (&v)[16], u32x4& z, u32x4 rs, unsigned voff, unsigned vz, int rot) {
  unsigned t;
  int n, so;
  asm volatile(G_LOOP(0, "v_max3_u32 %[t], v[188], v[192], v[196]\n\t" G_MAX(2) G_MAX(4) G_MAX(6) G_MAX(8) G_MAX(10) G_MAX(12) G_MAX(14),
                      "v_cmp_eq_u32 vcc, -1, v[188]\n\ts_mov_b64 exec, vcc\n\tbuffer_load_dwordx4 v[188:191], %[vz], %[rs], 0 offen sc1\n\t"
                      "s_mov_b64 exec, -1\n\t" G_RE0)
               : G_OPS("+", v), "+{v[188:191]}"(z), G_TMP
               : G_IN, [vz] "v"(vz)
               : "vcc", "scc", "memory");
}

DI unsigned off_o(int br, int r, int g, int w, int lane) {
  const int head = g * 4 + w, l15 = lane & 15, lg = lane >> 4;
  return WS_O + (br * 16 + r) * 131072 + (head >> 1) * 1024 + (head & 1) * 512 + (lg & 1) * 16384 + ((lg >> 1) * 16 + l15) * 16;
}
DI unsigned off_kv(int r, int g, int w, int lane) { return WS_KV + ((g * 4 + w) * 16 + r) * 1024 + lane * 16; }
DI unsigned off_z(int r, int g, int w, int lane) { return (lane & 15) == 0 ? WS_Z + ((g * 4 + w) * 16 + r) * 64 + (lane >> 4) * 16 : OOB; }
DI void store_sc1(u32x4 v, __amdgpu_buffer_rsrc_t rs, unsigned voff) { __builtin_amdgcn_raw_buffer_store_b128(v, rs, voff, 0, 16); }
DI unsigned flag_line(bool is_a, int r, int g, int lane) {
  return (!is_a && lane >= 32) ? WS_FK + g * 256 : WS_FO + ((is_a ? 0 : 16) + r) * 256;
}
DI u32x2 next_tag(u32x2 e) { return __builtin_bit_cast(u32x2, __builtin_bit_cast(u64, e) + TAG_STEP); }

struct LoadCtx {
  __amdgpu_buffer_rsrc_t rx, rw, ro;
  unsigned vx, vh, vw, vo;
  int j, k0, t0;
};
template <bool IS_A, int I, int E, int TH = 0> DI void issue_items(LAS char* lds, LoadCtx& c) {
  if constexpr (I < E) {
    constexpr int K = SchedOf<IS_A>::v.kind[I], N = SchedOf<IS_A>::v.idx[I], U = SchedOf<IS_A>::v.sub[I];
    if constexpr (K == K_H) DMA(c.rx, L_HALO + N * 4096 + c.j * 1024, c.vh, ((2 * N + (c.j >> 1) + c.k0) & 7) * 128, 0, X_AUX);
    if constexpr (K == K_X) DMA(c.rx, L_X + (N & 3) * 16384 + c.j * 4096, c.vx, ((N + c.k0) & 7) * 128 + U * 7168, U * 1024, X_AUX);
    if constexpr (K == K_W) DMA(c.rw, w_addr(IS_A, N, U) - U * 1024 + c.j * 1024, c.vw, ((N + 2 * c.k0) & 15) * 98304, U * 1024, W_AUX);
    if constexpr (K == K_U) {
      constexpr int UA = SchedOf<IS_A>::v.uaddr[N];
      DMA(c.ro, UA + c.j * 1024, c.vo, (((N >> 1) + c.t0) & 15) * 32768 + (N & 1) * 4096, 0, O_AUX);
    }
    if constexpr (TH > 0) asm volatile("s_waitcnt vmcnt(%0)" ::"i"(TH) : "memory");
    issue_items<IS_A, I + 1, E, TH>(lds, c);
  }
}
template <bool IS_A, int S> DI void loader_step(LAS char* lds, LoadCtx& c, u64* ts) {
  if constexpr (has_bar(S)) {
    asm volatile("s_waitcnt vmcnt(%0)" ::"i"(SchedOf<IS_A>::v.wait[S]) : "memory");
    asm volatile("s_barrier" ::: "memory");
    issue_items<IS_A, SchedOf<IS_A>::v.beg[S + 1], SchedOf<IS_A>::v.beg[S + 2]>(lds, c);
  }
  if constexpr (S < 16) loader_step<IS_A, S + 1>(lds, c, ts);
}
DI void wait_flags(u32x2 tag, u32x4 rs, unsigned voff) {
#pragma nounroll
  for (int spins = 0; spins < SPIN_CAP; ++spins) {
    u32x2 seen;
    asm volatile("buffer_load_dwordx2 %0, %1, %2, 0 offen sc1\n\ts_waitcnt vmcnt(0)" : "=v"(seen) : "v"(voff), "s"(rs) : "memory");
    if (__all(voff == OOB || (seen[0] == tag[0] && seen[1] == tag[1]))) break;
  }
}
template <bool IS_A>
DI void loader_path(const Params& P, LAS char* lds, const char* px, const char* pw, const char* po, int wave, int lane, int r, int g,
                    int k0, int t0) {
  LoadCtx c;
  const int j = wave - 4;
  c.j = j;
  c.k0 = k0;
  c.t0 = t0;
  c.rx = mkrsrc(px, 2048 * 1024);
  c.rw = mkrsrc(pw, 512 * 3072);
  const int row8 = lane >> 3, ch = lane & 7;
  const unsigned xsw = row8 * 1024 + ((ch ^ (row8 & 6)) * 16);
  c.vx = (r * 128 + j * 32) * 1024 + xsw;
  c.vh = (unsigned)((r * 128 - 16 + (j & 1) * 8) * 1024) + xsw;
  const int fsw = ((row8 >> 1) & 1) | ((j & 1) << 1);
  c.vw = (j * 8 + row8) * 3072 + g * 128 + (((ch >> 1) ^ fsw) * 32) + (ch & 1) * 16;
  issue_items<IS_A, SchedOf<IS_A>::v.beg[0], SchedOf<IS_A>::v.beg[1]>(lds, c);
  SCHED0();
  c.ro = mkrsrc(po, 512 * 1024);
  const int row4 = lane >> 4, c16 = lane & 15;
  c.vo = (j * 8 + row4) * 1024 + (g & 3) * 256 + (((c16 >> 1) ^ (row4 | ((j & 1) << 2))) * 32) + (c16 & 1) * 16;
  const u32x4 rws4 = mkrsrc4(P.ws, WS_USED);
  const unsigned line = flag_line(IS_A, r, g, lane);
  u32x2 tag;
  asm volatile("buffer_load_dwordx2 %0, %1, %2, 0 offen offset:128 sc1" : "=v"(tag) : "v"(line), "s"(rws4) : "memory");
  u64 ts[18] = {};
  loader_step<IS_A, 0>(lds, c, ts);
  asm volatile("" : "+v"(tag));
  tag = next_tag(tag);
  if constexpr (!IS_A) {
    if (j == 0) wait_flags(tag, rws4, lane >= 32 ? line + (lane & 15) * 8 : OOB);
    asm volatile("s_barrier" ::: "memory");
    issue_items<IS_A, SchedOf<IS_A>::v.beg[18], SchedOf<IS_A>::v.beg[19], THROTTLE>(lds, c);
  }
  if (j == 0) wait_flags(tag, rws4, lane < 32 ? line + (lane & 7) * 8 : OOB);
  asm volatile("s_waitcnt vmcnt(0)\n\ts_barrier" ::: "memory");
  if (j == 0) {
    const bool cl = lane == 0 ? g == 0 : (!IS_A && lane == 32 && r == 0);
    __builtin_amdgcn_raw_buffer_store_b64(tag, mkrsrc(P.ws, WS_USED), cl ? line + 128 : OOB, 0, 16);
  }
  STAMP(1);
  if constexpr (IS_A) asm volatile(".Lcode_end_la:");
  else asm volatile(".Lcode_end_lb:");
  __builtin_amdgcn_endpgm();
}

struct Acc {
  f32x4 q[8], k[9], v[9];
};
struct Frag {
  b16x8 x[9];
  b16x8 w[3];
};
struct Addr {
  unsigned x[2], h[2], w[2];
  __amdgpu_buffer_rsrc_t rws;
  u32x2 epoch;
  u32x4 any;
  unsigned o, kv, z;
  int wave, r, g;
};
template <bool IS_A, int S, int SEG> DI b16x8 read_w(const Addr& ad) {
  constexpr int A = w_addr(IS_A, S, SEG) - L_W;
  constexpr int HI = A >= W_HI;
  return cat8(trread(ad.w[HI] + (A - HI * W_HI)), trread(ad.w[HI] + (A - HI * W_HI + 512)));
}
template <int S> DI void read_x(Frag& f, const Addr& ad) {
#pragma unroll
  for (int b = 0; b < 8; ++b) f.x[b + 1] = ldsread(ad.x[S & 1] + (((S >> 1) & 3) * 16384 + b * 2048));
}
template <bool IS_A, int S> DI void read_frags(Frag& f, const Addr& ad) {
  if constexpr (IS_A || S < QD) f.w[0] = read_w<IS_A, S, 0>(ad);
  f.w[1] = read_w<IS_A, S, 1>(ad);
  f.w[2] = read_w<IS_A, S, 2>(ad);
  if constexpr (IS_A) f.x[0] = ldsread(ad.h[S & 1] + (S >> 1) * 2048);
  read_x<S>(f, ad);
}
template <bool IS_A, int S> DI void mma_step(Acc& a, const Frag& f) {
  if constexpr (IS_A) {
    a.k[0] = MFMA32(f.w[1], f.x[0], a.k[0]);
    a.v[0] = MFMA32(f.x[0], f.w[2], a.v[0]);
  }
#pragma unroll
  for (int b = 0; b < 8; ++b) {
    if constexpr (IS_A || S < QD) a.q[b] = MFMA32(f.w[0], f.x[b + 1], a.q[b]);
    if constexpr (IS_A) a.k[b + 1] = MFMA32(f.w[1], f.x[b + 1], a.k[b + 1]);
    else a.k[b + 1] = MFMA32(f.x[b + 1], f.w[1], a.k[b + 1]);
    a.v[b + 1] = MFMA32(f.x[b + 1], f.w[2], a.v[b + 1]);
  }
}
template <bool IS_A, int S> DI void gemm1_step(Acc& a, Frag& f0, Frag& f1, Addr& ad) {
  SCHED0();
  constexpr bool POST = S == 16 || (!IS_A && S == S_POST);
  constexpr int YOUNG = (S == 16 || CLR_START) ? 0 : clr_before(S) - clr_before(S_CLRK);
  if constexpr (POST) __builtin_amdgcn_s_waitcnt(0x0070 | YOUNG);
  else __builtin_amdgcn_s_waitcnt(0xc07f);
  if constexpr (has_bar(S)) asm volatile("s_barrier" ::: "memory");
  SCHED0();
  if constexpr (POST) {
    if (ad.wave == 0) {
      const int lane = fresh_lane();
      const unsigned line = flag_line(IS_A, ad.r, ad.g, lane);
      const unsigned at = S == 16 ? (lane == 0 ? line + ad.g * 8 : OOB) : (lane == 32 ? line + ad.r * 8 : OOB);
      __builtin_amdgcn_raw_buffer_store_b64(next_tag(ad.epoch), ad.rws, at, 0, 16);
    }
    SCHED0();
  }
  if constexpr (S <= 15) {
    constexpr int CI = CLR_START ? -1 : clr_index(S);
    constexpr int NST = CLR_START ? 0 : (CI >= 0 ? CLR_PER : 0) + (!IS_A && S == S_CLRK ? 2 : 0);
    read_frags<IS_A, S>((S & 1) ? f1 : f0, ad);
    if constexpr (NST > 0) {
      u32x4 sv = ad.any;
      sv[0] = SENT;
      if constexpr (!IS_A && S == S_CLRK) {
        store_sc1(sv, ad.rws, ad.kv);
        store_sc1(sv, ad.rws, ad.z);
      }
      if constexpr (CI >= 0) {
#pragma unroll
        for (int i = 0; i < CLR_PER; ++i) store_sc1(sv, ad.rws, ad.o + (CI * CLR_PER + i) * 32768);
      }
    }
    if constexpr (S >= 1) {
      mma_step<IS_A, S - 1>(a, (S & 1) ? f0 : f1);
      constexpr int NR = IS_A ? 15 : S < QD ? 14 : 12;
      constexpr int RPG = (!IS_A && S > QD) ? LATE_RPG : 1;
#pragma unroll
      for (int i = 0; i < NR / RPG; ++i) {
        SGB(0x8, 1);
        SGB(0x100, RPG);
      }
      if constexpr (NST > 0) {
#pragma unroll
        for (int i = 0; i < NST; ++i) {
          SGB(0x8, 1);
          SGB(0x2, 2);
          SGB(0x40, 1);
        }
      }
      SGB(0x8, 16);
    }
    gemm1_step<IS_A, S + 1>(a, f0, f1, ad);
  }
}
template <int S> DI void q_step(Acc& a, Frag& f0, Frag& f1, const Addr& ad) {
  Frag& fn = (S & 1) ? f1 : f0;
  Frag& fp = (S & 1) ? f0 : f1;
  if constexpr (S <= 13) {
    fn.w[0] = read_w<false, S, 0>(ad);
    read_x<S>(fn, ad);
  }
#pragma unroll
  for (int b = 0; b < 8; ++b) a.q[b] = MFMA32(fp.w[0], fp.x[b + 1], a.q[b]);
  if constexpr (S <= 13) {
    SGB(0x100, 2);
#pragma unroll
    for (int i = 0; i < 8; ++i) {
      SGB(0x8, 1);
      SGB(0x100, 1);
    }
  }
  SCHED0();
  if constexpr (S <= 13) q_step<S + 1>(a, f0, f1, ad);
}

template <bool IS_A, int T> DI void wo_read(b16x8 (&wf)[8], const unsigned (&ub)[3][8]) {
  constexpr int A0 = SchedOf<IS_A>::v.uaddr[2 * T], A1 = SchedOf<IS_A>::v.uaddr[2 * T + 1];
#pragma unroll
  for (int j = 0; j < 8; ++j) wf[j] = cat8(trread(ub[A0 >> 16][j] + (A0 & 65535)), trread(ub[A1 >> 16][j] + (A1 & 65535)));
}
template <bool IS_A, int T> DI void wo_pre(b16x8 (&wf)[WO_PRE + 1][8], const unsigned (&ub)[3][8]) {
  wo_read<IS_A, T>(wf[T], ub);
  if constexpr (T + 1 < WO_PRE) wo_pre<IS_A, T + 1>(wf, ub);
}
struct OGather {
  u32x4 rs;
  unsigned voff;
  int rot;
  int lane;
  GAS u64* dg;
};
template <bool IS_A, int T>
DI void gemm2_step(f32x4 (&c2)[8], b16x8 (&wf)[WO_PRE + 1][8], const unsigned (&ub)[3][8], u32x4 (&of)[16], const OGather& og) {
  if constexpr ((T & 3) == 0) {
    poll4<T / 4>(of, og.rs, og.voff, og.rot);
    SCHED0();
  }
  if constexpr (T + WO_PRE < 16) wo_read<IS_A, T + WO_PRE>(wf[(T + WO_PRE) % (WO_PRE + 1)], ub);
#pragma unroll
  for (int j = 0; j < 8; ++j) c2[j] = MFMA32(wf[T % (WO_PRE + 1)][j], __builtin_bit_cast(b16x8, of[T]), c2[j]);
  if constexpr (T + WO_PRE < 16) {
#pragma unroll
    for (int j = 0; j < 8; ++j) {
      SGB(0x8, 1);
      SGB(0x100, 2);
    }
  }
  SCHED0();
  if constexpr (T < 15) gemm2_step<IS_A, T + 1>(c2, wf, ub, of, og);
}

template <bool IS_A>
DI void compute_path(const Params& P, LAS char* lds, int wave, int lane, int r, int g, int t0, u64 cyc0) {
  constexpr int BR = IS_A ? 0 : 1;
  lane = fresh_lane();
  int l15 = lane & 15, lg = lane >> 4;
  const int head = g * 4 + wave;
  const __amdgpu_buffer_rsrc_t rws = mkrsrc(P.ws, WS_USED);
  const u32x4 rws4 = mkrsrc4(P.ws, WS_USED);
  const unsigned lbase = (unsigned)(size_t)lds;

  GAS u64* dgp = (GAS u64*)(P.ws + WS_DIAG) + (blockIdx.x * 8 + wave) * 32;
  asm volatile("" : "+s"(dgp));
  Acc a;
  const f32x4 zero = {0.f, 0.f, 0.f, 0.f};
  const f32x4 eps = {EPS, EPS, EPS, EPS};
#pragma unroll
  for (int b = 0; b < 8; ++b) a.q[b] = IS_A ? zero : eps;
#pragma unroll
  for (int b = 0; b < 9; ++b) { a.k[b] = IS_A ? zero : eps; a.v[b] = zero; }
  Frag f0, f1;
  Addr ad;
  {
    ad.epoch = __builtin_amdgcn_raw_buffer_load_b64(rws, flag_line(IS_A, r, g, lane) + 128, 0, 16);
    ad.o = off_o(BR, r, g, wave, lane);
    asm volatile("" : "+v"(ad.o));
    if constexpr (!IS_A) {
      ad.kv = off_kv(r, g, wave, lane);
      ad.z = off_z(r, g, wave, lane);
      asm volatile("" : "+v"(ad.kv), "+v"(ad.z));
    }
    ad.any = __builtin_nondeterministic_value(ad.any);
    ad.rws = rws; ad.wave = wave; ad.r = r; ad.g = g;
    const unsigned xb = lbase + l15 * 128 + ((lg ^ (l15 & 6)) * 16);
    ad.x[0] = L_X + xb;
    ad.x[1] = L_X + (xb ^ 64);
    ad.h[0] = L_HALO + xb;
    ad.h[1] = L_HALO + (xb ^ 64);
    const int q = l15 >> 2, p = l15 & 3;
    const int fsw = ((q >> 1) & 1) | ((lg & 1) << 1);
    ad.w[0] = lbase + L_W + lg * 1024 + q * 128 + ((wave ^ fsw) * 32) + p * 8;
    ad.w[1] = ad.w[0] + W_HI;
    asm volatile("" : "+v"(ad.w[0]), "+v"(ad.w[1]));
    if constexpr (IS_A) asm volatile("" : "+v"(ad.h[0]), "+v"(ad.h[1]));
  }
  gemm1_step<IS_A, 0>(a, f0, f1, ad);
  asm volatile("" : "+v"(ad.o));
  lane = fresh_lane();
  l15 = lane & 15;
  lg = lane >> 4;
  STAMP(1);
  const s16x8 ones_s = {0x3F80, 0x3F80, 0x3F80, 0x3F80, 0x3F80, 0x3F80, 0x3F80, 0x3F80};
  const b16x8 ones = __builtin_bit_cast(b16x8, ones_s);

  if constexpr (IS_A) {
    s16x4 kf[9], vf[9], qf[8];
#pragma unroll
    for (int b = 0; b < 9; ++b) a.k[b] = MFMA32(f1.w[1], f1.x[b], a.k[b]);
#pragma unroll
    for (int b = 0; b < 8; ++b) a.q[b] = MFMA32(f1.w[0], f1.x[b + 1], a.q[b]);
#pragma unroll
    for (int b = 0; b < 9; ++b) a.v[b] = MFMA32(f1.x[b], f1.w[2], a.v[b]);
    const float NEG = -__builtin_inff();
    f32x4 cprev, ccur;
    unsigned sel[4];
#pragma unroll
    for (int i = 0; i < 4; ++i) {
      const int c = 4 * lg + i;
      const bool par = ((c ^ l15) & 1) == 0;
      cprev[i] = (par && c > l15) ? 0.f : NEG;
      ccur[i] = (par && c <= l15) ? 0.f : NEG;
    }
    {
      const unsigned s0 = (l15 & 1) ? 0x01000c0cu : 0x0c0c0100u, s1 = (l15 & 1) ? 0x03020c0cu : 0x0c0c0302u, sz = 0x0c0c0c0cu;
      const int cA = 4 * lg + (l15 & 1), cB = cA + 2;
      sel[0] = cA > l15 ? s0 : sz;
      sel[1] = cB > l15 ? s1 : sz;
      sel[2] = cA > l15 ? sz : s0;
      sel[3] = cB > l15 ? sz : s1;
    }
    f32x4 cfirst = cprev;
    if (r == 0) cfirst = (f32x4){NEG, NEG, NEG, NEG};
    const unsigned oofs = ad.o;
#pragma unroll
    for (int b = 0; b < 9; ++b) kf[b] = pk4(a.k[b]);
#pragma unroll
    for (int b = 0; b < 8; ++b) qf[b] = pk4(a.q[b]);
#pragma unroll
    for (int i = 0; i < 26; ++i) {
      SGB(0x8, 1);
      SGB(0x2, 2);
    }
    SCHED0();
    f32x4 s0[8], s1[8];
#pragma unroll
    for (int b = 0; b < 8; ++b) {
      s0[b] = MFMA16(kf[b], qf[b], b == 0 ? cfirst : cprev);
      s1[b] = MFMA16(kf[b + 1], qf[b], ccur);
    }
#pragma unroll
    for (int b = 0; b < 9; ++b) vf[b] = pk4(a.v[b]);
#pragma unroll
    for (int i = 0; i < 16; ++i) {
      SGB(0x8, 1);
      SGB(0x2, 1);
    }
    SCHED0();
    const float C1 = 0.25f * LOG2E;
    float t0v[8], t1v[8], mc[8];
#pragma unroll
    for (int b = 0; b < 8; ++b) {
      t0v[b] = fmx(fmx(s0[b][0], s0[b][1]), fmx(s1[b][0], s1[b][1]));
      t1v[b] = fmx(fmx(s0[b][2], s0[b][3]), fmx(s1[b][2], s1[b][3]));
      mc[b] = fmx(t0v[b], t1v[b]);
    }
#pragma unroll
    for (int b = 0; b < 8; b += 4) {
      const f32x2 ab = swap16(mc[b], mc[b + 1]), cd = swap16(mc[b + 2], mc[b + 3]);
      const f32x2 w2 = swap32(fmx(ab[0], ab[1]), fmx(cd[0], cd[1]));
      const float w = fmx(w2[0], w2[1]) * C1;
      const f32x2 h2 = swap32(w, w);
      const f32x2 m01 = swap16(h2[0], h2[0]), m23 = swap16(h2[1], h2[1]);
      mc[b] = m01[0]; mc[b + 1] = m01[1]; mc[b + 2] = m23[0]; mc[b + 3] = m23[1];
    }
    SCHED0();
    f32x4 o[8], sm[8];
#pragma unroll
    for (int b = 0; b < 8; ++b) {
      const float e0 = __builtin_amdgcn_exp2f(__builtin_fmaf(t0v[b], C1, -mc[b]));
      const float e1 = __builtin_amdgcn_exp2f(__builtin_fmaf(t1v[b], C1, -mc[b]));
      const unsigned p = pk2(e0, e1);
      const u32x4 pu = {__builtin_amdgcn_perm(p, p, sel[0]), __builtin_amdgcn_perm(p, p, sel[1]), __builtin_amdgcn_perm(p, p, sel[2]),
                        __builtin_amdgcn_perm(p, p, sel[3])};
      const b16x8 pf = __builtin_bit_cast(b16x8, pu);
      sm[b] = MFMA32(ones, pf, zero);
      o[b] = MFMA32(cat8(vf[b], vf[b + 1]), pf, zero);
    }
    SCHED0();
    s16x4 ob[8];
#pragma unroll
    for (int b = 0; b < 8; ++b) {
      ob[b] = pk4(o[b] * __builtin_amdgcn_rcpf(sm[b][0]));
      if (b & 1) __builtin_amdgcn_raw_buffer_store_b128(no_sent(pair_granule(ob[b - 1], ob[b])), rws, oofs + (b >> 1) * 32768, 0, IS_A ? PUB_A : PUB_B);
    }
    STAMP(4);
  } else {
#pragma unroll
    for (int b = 1; b < 9; ++b) a.k[b] = MFMA32(f1.x[b], f1.w[1], a.k[b]);
#pragma unroll
    for (int b = 1; b < 9; ++b) a.v[b] = MFMA32(f1.x[b], f1.w[2], a.v[b]);
    f0.w[0] = read_w<false, 14, 0>(ad);
    f1.w[0] = read_w<false, 15, 0>(ad);
    f32x4 kv = zero, zz = zero;
#pragma unroll
    for (int i = 0; i < 4; ++i) {
      const b16x8 pkf = cat8(phi4(a.k[2 * i + 1]), phi4(a.k[2 * i + 2]));
      const b16x8 vf = cat8(pk4(a.v[2 * i + 1]), pk4(a.v[2 * i + 2]));
      kv = MFMA32(pkf, vf, kv);
      zz = MFMA32(pkf, ones, zz);
    }
    SGB(0x100, 4);
#pragma unroll
    for (int i = 0; i < 16; ++i) {
      SGB(0x8, 1);
      SGB(0x2, 2);
    }
    store_sc1(no_sent(__builtin_bit_cast(u32x4, kv)), rws, ad.kv);
    store_sc1(no_sent(__builtin_bit_cast(u32x4, zz)), rws, ad.z);
    SCHED0();
    STAMP(2);
#pragma unroll
    for (int b = 0; b < 8; ++b) a.q[b] = MFMA32(f0.w[0], f0.x[b + 1], a.q[b]);
    SCHED0();
    q_step<QD>(a, f0, f1, ad);
    STAMP(9);
    u32x4 part[16], zp;
    const unsigned vk = WS_KV + head * 16384 + lane * 16, vz = WS_Z + head * 1024 + l15 * 64 + lg * 16;
    const int krot = MAP84 ? (r & 7) * 2048 : (r & 3) * 4096;
    s16x4 pq[8];
#pragma unroll
    for (int b = 0; b < 8; ++b) {
      if (b == PHI_EARLY) {
        BARRIER();
        gather1(zp, rws4, vz);
        gather16(part, rws4, vk, krot);
        SCHED0();
      }
      pq[b] = phi4(a.q[b]);
      asm volatile("" : "+v"(pq[b]));
    }
    if (PHI_EARLY == 8) {
      BARRIER();
      gather1(zp, rws4, vz);
      gather16(part, rws4, vk, krot);
    }
    const unsigned oofs = ad.o;
    SCHED0();
    poll17(part, zp, rws4, vk, vz, krot);
    STAMP(3);
    f32x4 pf[16];
#pragma unroll
    for (int i = 0; i < 16; ++i) pf[i] = __builtin_bit_cast(f32x4, part[i]);
#pragma unroll
    for (int st = 1; st < 16; st <<= 1)
#pragma unroll
      for (int i = 0; i < 16; i += 2 * st) pf[i] += pf[i + st];
    f32x4 zf = __builtin_bit_cast(f32x4, zp);
#pragma unroll
    for (int i = 0; i < 4; ++i) zf[i] = row16_sum(zf[i]);
    const s16x4 kvb = pk4(pf[0]), zb = pk4(zf);
    f32x4 num[8], den[8];
    SCHED0();
#pragma unroll
    for (int b = 0; b < 8; ++b) {
      den[b] = MFMA16(zb, pq[b], eps);
      num[b] = MFMA16(kvb, pq[b], zero);
    }
    SCHED0();
    s16x4 ob[8];
#pragma unroll
    for (int b = 0; b < 8; ++b) {
      ob[b] = pk4(num[b] * __builtin_amdgcn_rcpf(den[b][0]));
      if (b & 1) __builtin_amdgcn_raw_buffer_store_b128(no_sent(pair_granule(ob[b - 1], ob[b])), rws, oofs + (b >> 1) * 32768, 0, IS_A ? PUB_A : PUB_B);
    }
    STAMP(4);
  }
  SCHED0();
  const int blk = (g >> 2) * 4 + wave;
  unsigned ub[3][8];
  {
    const int q = l15 >> 2, p = l15 & 3;
    const int sw = q | ((lg & 1) << 2);
    const unsigned u0 = lbase + lg * 1024 + q * 256 + p * 8;
#pragma unroll
    for (int w = 0; w < (IS_A ? 2 : 3); ++w)
#pragma unroll
      for (int j = 0; j < 8; ++j) {
        ub[w][j] = u0 + ((j ^ sw) * 32) + w * 65536;
        asm volatile("" : "+v"(ub[w][j]));
      }
  }
  u32x4 of[16];
  OGather og;
  og.rs = rws4;
  og.voff = WS_O + (BR * 16 + r) * 131072 + blk * 16384 + lane * 16;
  og.rot = t0 * 1024;
  og.lane = lane;
  og.dg = dgp;
  SCHED0();
  __builtin_amdgcn_s_sleep(IS_A ? O_DELAY : O_DELAY_B);
  BARRIER();
  STAMP(5);
  gather16(of, og.rs, og.voff, og.rot);
  b16x8 wf[WO_PRE + 1][8];
  wo_pre<IS_A, 0>(wf, ub);
  SCHED0();
  f32x4 c2[8];
#pragma unroll
  for (int j = 0; j < 8; ++j) c2[j] = zero;
  SCHED0();
  gemm2_step<IS_A, 0>(c2, wf, ub, of, og);
  lane = fresh_lane();
  l15 = lane & 15;
  lg = lane >> 4;
  STAMP(7);
  {
    const __amdgpu_buffer_rsrc_t rout = mkrsrc(P.out, 2048 * 2048);
    const unsigned row = r * 128 + blk * 16 + l15;
    const unsigned ob = row * 2048 + BR * 1024 + (g & 3) * 256 + ((lg & 1) * 32 + (lg >> 1) * 16);
#pragma unroll
    for (int j = 0; j < 8; j += 2)
      __builtin_amdgcn_raw_buffer_store_b128(pair_granule(pk4(c2[j]), pk4(c2[j + 1])), rout, ob + j * 32, 0, OUT_AUX);
  }
  STAMP(8);
  if constexpr (IS_A) asm volatile(".Lcode_end_a:");
  else asm volatile(".Lcode_end_b:");
  __builtin_amdgcn_endpgm();
}

__global__ __launch_bounds__(512, 2) void fused_kernel(const char* xa, const char* wa, const char* xb, const char* wb, char* ws,
                                                       const char* oa, const char* ob, char* out) {
  extern __shared__ __attribute__((aligned(16))) char lds_raw[];
  LAS char* lds = (LAS char*)lds_raw;
  const int wave = __builtin_amdgcn_readfirstlane(threadIdx.x >> 6);
  const int lane = threadIdx.x & 63;
  const int bid = blockIdx.x;
  const int xcd = bid & 7, idx = bid >> 3;
  const int br = xcd >> 2;
  const int r = (xcd & 3) * 4 + (idx >> 3), g = idx & 7;
  u64 cyc0 = 0;
  const char* px = br ? xb : xa;
  const char* pw = br ? wb : wa;
  const char* po = br ? ob : oa;
  Params P;
  P.ws = ws;
  P.out = out;
  const int k0 = (g + 2 * r) & 7;
  const int t0 = ROT ? (((g >> 2) + 2 * r) * 2 + (g & 3) * 4) & 15 : 0;
  {
    u64 pc;
    int len;
    asm volatile("s_mov_b32 m0, %2\n\ts_getpc_b64 %0\n.Lcode_ref:\n\ts_max_i32 %1, .Lcode_end_a-.Lcode_ref, 0\n\t"
                 "s_max_i32 %1, .Lcode_end_b-.Lcode_ref, %1\n\ts_max_i32 %1, .Lcode_end_la-.Lcode_ref, %1\n\t"
                 "s_max_i32 %1, .Lcode_end_lb-.Lcode_ref, %1"
                 : "=s"(pc), "=s"(len) : "s"((unsigned)(size_t)(lds + L_SP) + wave * 256) : "scc", "memory");
    asm volatile("buffer_load_dword %0, %1, 0 offen lds" ::"v"(idx * 2048 + wave * 256 + (lane & 1) * 128), "s"(mkrsrc4((const void*)pc, len))
                 : "memory");
  }
  if (wave < 4) {
    if (br == 0) compute_path<true>(P, lds, wave, lane, r, g, t0, cyc0);
    else compute_path<false>(P, lds, wave, lane, r, g, t0, cyc0);
  } else {
    if (br == 0) loader_path<true>(P, lds, px, pw, po, wave, lane, r, g, k0, t0);
    else loader_path<false>(P, lds, px, pw, po, wave, lane, r, g, k0, t0);
  }
}

extern "C" void kernel_launch(void* const* d_in, const int* in_sizes, int n_in, void* d_out, int out_size, void* d_ws,
                              size_t ws_size, hipStream_t stream) {
  hipFuncSetAttribute((const void*)fused_kernel, hipFuncAttributeMaxDynamicSharedMemorySize, LDS_BYTES);
  hipLaunchKernelGGL(fused_kernel, dim3(256), dim3(512), LDS_BYTES, stream, (const char*)d_in[0], (const char*)d_in[1],
                     (const char*)d_in[3], (const char*)d_in[4], (char*)d_ws, (const char*)d_in[2], (const char*)d_in[5], (char*)d_out);
}
```

```cpp
#include <hip/hip_runtime.h>
#include <hip/hip_bf16.h>

typedef float f32x4 __attribute__((ext_vector_type(4)));
typedef float f32x2 __attribute__((ext_vector_type(2)));
typedef __bf16 b16x8 __attribute__((ext_vector_type(8)));
typedef __bf16 b16x2 __attribute__((ext_vector_type(2)));
typedef short s16x4 __attribute__((ext_vector_type(4)));
typedef short s16x8 __attribute__((ext_vector_type(8)));
typedef unsigned u32x4 __attribute__((ext_vector_type(4)));
typedef unsigned u32x2 __attribute__((ext_vector_type(2)));
typedef unsigned long long u64;
#define LAS __attribute__((address_space(3)))
#define DI __device__ __forceinline__

constexpr int PRO_BUDGET = 21;
constexpr int STEP_BUDGET = 6;
constexpr int WQ_MIN = 4;
constexpr int U_HEAD = 4;
constexpr int U_MID = 10;
constexpr int U_FIRST = 10;
constexpr int U_REST = 14;
constexpr int BAR_MASK = 0x15557;
constexpr int S_POST = 10;
constexpr int CLR_A0 = 12;
constexpr int CLR_B0 = 4;
constexpr int CLR_B1 = 6;
constexpr int S_CLRK = 3;
constexpr int O_DELAY = 24;
constexpr int O_DELAY_B = 20;
constexpr int O_DELAY3 = 14;
constexpr int OLA = 6;
constexpr int KTAIL = 2;
constexpr int QG = 8;
constexpr int T_AT = 14;
constexpr int PF_C1 = 2;

constexpr int L_W = 65536;
constexpr int L_HALO = 139264;
constexpr int L_SP = 155648;
constexpr int LDS_BYTES = 163840;
constexpr int W_HI = 61440;
constexpr int QD = 4;

constexpr unsigned WS_O = 0;
constexpr unsigned WS_KV = 4u << 20;
constexpr unsigned WS_Z = WS_KV + (512u << 10);
constexpr unsigned WS_FO = WS_Z + (32u << 10);
constexpr unsigned WS_FK = WS_FO + (8u << 10);
constexpr unsigned WS_USED = WS_FK + (8u << 10);
constexpr u64 TAG_STEP = 0x9E3779B97F4A7C15ull;
constexpr unsigned OOB = 0x80000000u;
constexpr unsigned SENT = 0xFFFFFFFFu;
constexpr int SPIN_CAP = 1 << 18;

constexpr bool has_bar(int s) { return s > 16 || ((BAR_MASK >> s) & 1); }
constexpr int bar_le(int s) { while (!has_bar(s)) --s; return s; }
constexpr int bar_gt(int s) { ++s; while (!has_bar(s)) ++s; return s; }
static_assert(has_bar(0) && has_bar(16) && has_bar(S_POST) && S_CLRK < S_POST && has_bar(WQ_MIN) && has_bar(U_FIRST) && has_bar(U_REST), "barriers");
constexpr int clr_cnt(bool is_a, int s) { return is_a ? (s == CLR_A0 ? 4 : 0) : (s == CLR_B0 || s == CLR_B1 ? 2 : 0); }
constexpr int clr_before(bool is_a, int s) { int n = 0; for (int i = 0; i < s; ++i) n += clr_cnt(is_a, i); return n; }
static_assert(clr_before(true, 16) == 4 && clr_before(false, S_POST) == 4, "sentinel stores");
constexpr int x_addr(bool is_a, int c) {
  constexpr int u[8] = {0, 4, 8, 12, 22, 26, 0, 4};
  return is_a ? (c & 3) * 16384 : u[c] * 4096;
}
constexpr int w_addr(bool is_a, int s, int seg) {
  constexpr int u[3][12] = {{30, 31, 32, 33, 16, 17, 18, 19, 20, 21, 34, 38},
                            {16, 18, 20, 35, 37, 30, 32, 16, 18, 20, 35, 37},
                            {17, 19, 21, 36, 39, 31, 33, 17, 19, 21, 36, 39}};
  return (is_a || s < QD) ? L_W + ((s + 2) % 6) * 12288 + seg * 4096 : u[seg][s - QD] * 4096;
}
constexpr int q_phase(int s) { return (s >= 14 || s < QG) ? 16 : 17; }

enum { K_H, K_X, K_W, K_U, K_T };
constexpr int MAXIT = 124;
struct Sched {
  int n;
  int kind[MAXIT], idx[MAXIT], sub[MAXIT], need[MAXIT], last[MAXIT], lo[MAXIT], hi[MAXIT], iv[MAXIT];
  int beg[23];
  int wait[18];
  int uaddr[32];
  int oaddr[16];
  int xch;
  constexpr void add(int k, int i, int s, int nd, int ls, int addr, int units) {
    kind[n] = k; idx[n] = i; sub[n] = s; need[n] = nd; last[n] = ls; lo[n] = addr / 4096; hi[n] = addr / 4096 + units; ++n;
  }
  constexpr void swap(int a, int b) {
    int* f[8] = {kind, idx, sub, need, last, lo, hi, iv};
    for (int i = 0; i < 8; ++i) { const int t = f[i][a]; f[i][a] = f[i][b]; f[i][b] = t; }
  }
};
constexpr Sched make_sched(bool is_a) {
  Sched sc{};
  for (int s = 0; s < 16; s += 2) {
    const bool xlast = s == 2 && !has_bar(3);
    if (is_a ? (s & 3) == 0 : s == 0) sc.add(K_H, s >> 2, 0, s, is_a ? s + 3 : -1, L_HALO + (s >> 2) * 4096, 1);
    for (int p = 0; p < 3; ++p) {
      if (p == (xlast ? 2 : 0)) {
        for (int i = 0; i < 4; ++i) sc.add(K_X, s >> 1, i, s, (is_a || s < QD || s == 14) ? s + 1 : 19, x_addr(is_a, s >> 1), 4);
      } else {
        const int t = s + (xlast ? p : p - 1);
        for (int i = (!is_a && t >= QD) ? 1 : 0; i < 3; ++i) sc.add(K_W, t, i, t, t, w_addr(is_a, t, i), 1);
      }
    }
  }
  if (!is_a) {
    for (int i = 0; i < 16 - QD; ++i) {
      const int s = i < 2 ? 14 + i : QD + i - 2;
      sc.add(K_W, s, 0, q_phase(s), q_phase(s), w_addr(is_a, s, 0), 1);
    }
    sc.add(K_T, 0, 0, 98, -1, 0, 0);
  }
  sc.add(K_T, 1, 0, 98, -1, 0, 0);
  const int scratch_last = is_a ? 15 : S_POST - 1;
  int busy[40] = {};
  for (int u = 0; u < 40; ++u) {
    busy[u] = u == L_SP / 4096 ? scratch_last : -1;
    for (int i = 0; i < sc.n; ++i)
      if (sc.lo[i] <= u && u < sc.hi[i]) {
        const int ls = (sc.last[i] == 19 && ((u - sc.lo[i]) & 1) == 0) ? 18 : sc.last[i];
        if (ls > busy[u]) busy[u] = ls;
      }
  }
  for (int u = 39; u >= 0 && !is_a; --u)
    if (busy[u] == 18) { busy[u] = 20; sc.xch = u * 4096; break; }
  int nu = 0;
  for (int rl = -1; rl <= 20; ++rl)
    for (int u = 0; u < 40; ++u)
      if (busy[u] == rl) {
        if (nu < 32) { sc.uaddr[nu] = u * 4096; sc.add(K_U, nu, 0, 99, 99, u * 4096, 1); }
        else sc.oaddr[nu - 32] = u * 4096;
        ++nu;
      }
  for (int t = 8; t < 16; ++t) sc.oaddr[t] = sc.uaddr[t];
  int cur = -1, used = 0;
  for (int i = 0; i < sc.n; ++i) {
    int rel = (sc.lo[i] <= L_SP / 4096 && L_SP / 4096 < sc.hi[i]) ? scratch_last : -1;
    for (int k = 0; k < sc.n; ++k)
      if (sc.need[k] < sc.need[i] && sc.lo[k] < sc.hi[i] && sc.lo[i] < sc.hi[k] && sc.last[k] > rel) rel = sc.last[k];
    if (sc.kind[i] == K_U) rel = busy[sc.lo[i]];
    const int av = rel < 0 ? -1 : bar_gt(rel);
    if (sc.need[i] >= 16) {
      const int lo = sc.kind[i] == K_T ? (sc.idx[i] ? 0 : T_AT) : sc.kind[i] == K_W ? WQ_MIN : sc.idx[i] < U_HEAD ? U_FIRST : sc.idx[i] < U_HEAD + U_MID ? U_REST : 16;
      sc.iv[i] = av > lo ? av : lo;
      continue;
    }
    for (;;) {
      const int bud = cur < 0 ? PRO_BUDGET : (bar_gt(cur) - cur) * STEP_BUDGET;
      if (av <= cur && used < bud) break;
      cur = bar_gt(cur);
      used = 0;
    }
    sc.iv[i] = cur;
    ++used;
  }
  for (int i = 1; i < sc.n; ++i)
    for (int k = i; k > 0 && sc.iv[k - 1] > sc.iv[k]; --k) sc.swap(k - 1, k);
  for (int s = -1; s <= 21; ++s) {
    int b = 0;
    while (b < sc.n && sc.iv[b] < s) ++b;
    sc.beg[s + 1] = b;
  }
  for (int s = 0; s <= 17; ++s) {
    int last = 0;
    for (int i = 0; i < sc.n; ++i)
      if (sc.need[i] <= 17 && bar_le(sc.need[i]) <= s) last = i + 1;
    sc.wait[s] = sc.beg[s + 1] - last;
  }
  return sc;
}
template <bool IS_A> struct SchedOf { static constexpr Sched v = make_sched(IS_A); };
constexpr bool sched_ok(const Sched& sc, bool is_a) {
  for (int s = 0; s <= 17; ++s)
    if (has_bar(s) && (sc.wait[s] < 0 || sc.wait[s] > 63)) return false;
  int nu = 0;
  for (int i = 0; i < sc.n; ++i) {
    nu += sc.kind[i] == K_U;
    if (sc.iv[i] > (is_a ? 16 : 20)) return false;
    for (int k = 0; k < sc.n; ++k)
      if (k != i && sc.need[k] == sc.need[i] && sc.kind[k] != K_X && sc.lo[k] < sc.hi[i] && sc.lo[i] < sc.hi[k]) return false;
  }
  return nu == 32 && sc.n <= MAXIT;
}
static_assert(sched_ok(SchedOf<true>::v, true) && sched_ok(SchedOf<false>::v, false), "schedule");
constexpr int common_items() {
  const Sched &a = SchedOf<true>::v, &b = SchedOf<false>::v;
  int n = 0;
  while (n < a.beg[1] && n < b.beg[1] && a.kind[n] == b.kind[n] && a.idx[n] == b.idx[n] && a.sub[n] == b.sub[n] && a.lo[n] == b.lo[n]) ++n;
  return n;
}

struct Params {
  char* ws;
  char* out;
  const char* w;
  const char* x;
  int idx, k3;
};

DI __amdgpu_buffer_rsrc_t mkrsrc(const void* p, unsigned bytes) {
  return __builtin_amdgcn_make_buffer_rsrc((void*)p, 0, (int)bytes, 0x00020000);
}
DI u32x4 mkrsrc4(const void* p, unsigned bytes) {
  const u64 a = (u64)p;
  return (u32x4){(unsigned)a, (unsigned)(a >> 32) & 0xFFFFu, bytes, 0x00020000u};
}
#define DMA(rs, ldsoff, voff, soff, imm) \
  __builtin_amdgcn_raw_ptr_buffer_load_lds((rs), (LAS void*)(lds + (ldsoff)), 16, (voff), (soff), (imm), 0)
#define MFMA32(a, b, c) __builtin_amdgcn_mfma_f32_16x16x32_bf16((a), (b), (c), 0, 0, 0)
#define MFMA32_IP(a, b, c) asm volatile("v_mfma_f32_16x16x32_bf16 %0, %1, %2, %0" : "+v"(c) : "v"(a), "v"(b))
#define MFMA16(a, b, c) __builtin_amdgcn_mfma_f32_16x16x16bf16_1k((a), (b), (c), 0, 0, 0)
#define SCHED0() __builtin_amdgcn_sched_barrier(0)
#define SGB(mask, n) __builtin_amdgcn_sched_group_barrier((mask), (n), 0)
#define BARRIER() do { SCHED0(); asm volatile("s_barrier" ::: "memory"); SCHED0(); } while (0)

DI int fresh_lane() {
  int l;
  asm volatile("v_mbcnt_lo_u32_b32 %0, -1, 0\n\tv_mbcnt_hi_u32_b32 %0, -1, %0" : "=v"(l));
  return l;
}
DI unsigned pk2(float a, float b) {
  f32x2 f = {a, b};
  return __builtin_bit_cast(unsigned, __builtin_convertvector(f, b16x2));
}
DI s16x4 pk4(f32x4 v) {
  u32x2 u = {pk2(v[0], v[1]), pk2(v[2], v[3])};
  return __builtin_bit_cast(s16x4, u);
}
DI b16x8 cat8(s16x4 a, s16x4 b) {
  return __builtin_bit_cast(b16x8, __builtin_shufflevector(a, b, 0, 1, 2, 3, 4, 5, 6, 7));
}
DI s16x4 trread(unsigned a) { return __builtin_amdgcn_ds_read_tr16_b64_v4i16((LAS s16x4*)(size_t)a); }
DI b16x8 ldsread(unsigned a) { return *(LAS b16x8*)(size_t)a; }
DI float fmx(float a, float b) { return __builtin_elementwise_maximum(a, b); }
constexpr float LOG2E = 1.44269504088896f;
constexpr float EPS = 1e-6f;
template <int CTRL> DI float dpp_add(float x) {
  return x + __builtin_bit_cast(float, __builtin_amdgcn_update_dpp(0, __builtin_bit_cast(int, x), CTRL, 0xf, 0xf, true));
}
DI f32x2 swap16(float a, float b) {
  auto u = __builtin_amdgcn_permlane16_swap(__float_as_uint(a), __float_as_uint(b), false, false);
  return (f32x2){__uint_as_float(u[0]), __uint_as_float(u[1])};
}
DI f32x2 swap32(float a, float b) {
  auto u = __builtin_amdgcn_permlane32_swap(__float_as_uint(a), __float_as_uint(b), false, false);
  return (f32x2){__uint_as_float(u[0]), __uint_as_float(u[1])};
}
DI u32x4 pair_granule(s16x4 a, s16x4 b) {
  const u32x2 lo = __builtin_bit_cast(u32x2, a), hi = __builtin_bit_cast(u32x2, b);
  auto s0 = __builtin_amdgcn_permlane16_swap(lo[0], hi[0], false, false);
  auto s1 = __builtin_amdgcn_permlane16_swap(lo[1], hi[1], false, false);
  return (u32x4){s0[0], s1[0], s0[1], s1[1]};
}
DI u32x4 no_sent(u32x4 v) {
  v[0] = v[0] < SENT - 1 ? v[0] : SENT - 1;
  return v;
}
#define G_LD(i)                                                                         \
  "s_add_i32 %[so], %[rot], " #i "*1024\n\ts_and_b32 %[so], %[so], %[mk]\n\t"          \
  "buffer_load_dwordx4 v[192+4*" #i ":195+4*" #i "], %[vo], %[rs], %[so] offen sc1\n\t"
#define G_RE(i) "v_cmp_eq_u32 vcc, -1, v[192+4*" #i "]\n\ts_mov_b64 exec, vcc\n\t" G_LD(i) "s_mov_b64 exec, -1\n\t"
#define G_MAX(i) "v_max3_u32 %[t], v[192+4*" #i "], v[196+4*" #i "], %[t]\n\t"
#define G_LOOP(W, CHECK, RELOAD)                                                                                        \
  "s_waitcnt vmcnt(" W ")\n\ts_mov_b32 %[n], %[cap]\n.Lpoll%=:\n\t" CHECK                                              \
  "v_cmp_eq_u32 vcc, -1, %[t]\n\ts_cbranch_vccz .Ldone%=\n\ts_waitcnt vmcnt(0)\n\t" RELOAD                              \
  "s_waitcnt vmcnt(0)\n\ts_sub_u32 %[n], %[n], 1\n\ts_cbranch_scc0 .Lpoll%=\n.Ldone%=:"
#define G_TMP [t] "=&v"(t), [n] "=&s"(n), [so] "=&s"(so)
#define G_IN8 [vo] "v"(voff), [rs] "s"(rs), [rot] "s"(rot), [cap] "i"(SPIN_CAP), [mk] "i"(0x1c00), [vz] "v"(vz)
#define G_OPS8(c, v, z) c "{v[192:195]}"(v[0]), c "{v[196:199]}"(v[1]), c "{v[200:203]}"(v[2]), c "{v[204:207]}"(v[3]), \
                        c "{v[208:211]}"(v[4]), c "{v[212:215]}"(v[5]), c "{v[216:219]}"(v[6]), c "{v[220:223]}"(v[7]), c "{v[188:191]}"(z)
#define G_LDZ "buffer_load_dwordx4 v[188:191], %[vz], %[rs], 0 offen sc1\n\t"
DI void gather9(u32x4 (&v)[8], u32x4& z, u32x4 rs, unsigned voff, unsigned vz, int rot) {
  int so;
  asm volatile(G_LDZ G_LD(0) G_LD(1) G_LD(2) G_LD(3) G_LD(4) G_LD(5) G_LD(6) G_LD(7)
               : G_OPS8("=&", v, z), [so] "=&s"(so)
               : G_IN8
               : "scc", "memory");
}
template <int W>
DI void poll9(u32x4 (&v)[8], u32x4& z, u32x4 rs, unsigned voff, unsigned vz, int rot) {
  unsigned t;
  int n, so;
  asm volatile(G_LOOP("%[w]", "v_max3_u32 %[t], v[188], v[192], v[196]\n\t" G_MAX(2) G_MAX(4) G_MAX(6),
                      "v_cmp_eq_u32 vcc, -1, v[188]\n\ts_mov_b64 exec, vcc\n\t" G_LDZ "s_mov_b64 exec, -1\n\t" G_RE(0) G_RE(1) G_RE(2) G_RE(3)
                          G_RE(4) G_RE(5) G_RE(6) G_RE(7))
               : G_OPS8("+", v, z), G_TMP
               : G_IN8, [w] "i"(W)
               : "vcc", "scc", "memory");
}

DI unsigned off_o(int br, int r, int g, int w, int lane) {
  const int head = g * 4 + w, l15 = lane & 15, lg = lane >> 4;
  if (br) return WS_O + (16 + r) * 131072 + (w >> 1) * 65536 + (2 * g + (w & 1)) * 1024 + lane * 16;
  return WS_O + (br * 16 + r) * 131072 + (head >> 1) * 1024 + (head & 1) * 512 + (lg & 1) * 16384 + ((lg >> 1) * 16 + l15) * 16;
}
DI unsigned off_kv(int r, int g, int w, int lane) {
  return (lane & 16) ? OOB : WS_KV + ((g * 4 + w) * 16 + r) * 512 + (lane >> 5) * 256 + (lane & 15) * 16;
}
DI unsigned off_z(int r, int g, int w, int lane) { return (lane & 15) == 0 ? WS_Z + ((g * 4 + w) * 16 + r) * 64 + (lane >> 4) * 16 : OOB; }
DI void store_sc1(u32x4 v, __amdgpu_buffer_rsrc_t rs, unsigned voff) { __builtin_amdgcn_raw_buffer_store_b128(v, rs, voff, 0, 16); }
DI unsigned flag_line(bool is_a, int r, int g, int lane) {
  return (!is_a && lane >= 32) ? WS_FK + g * 256 : WS_FO + ((is_a && lane < 32 ? 0 : 16) + r) * 256;
}
DI u32x2 next_tag(u32x2 e) { return __builtin_bit_cast(u32x2, __builtin_bit_cast(u64, e) + TAG_STEP); }

struct LoadCtx {
  __amdgpu_buffer_rsrc_t rx, rw, ro;
  u32x4 ro4, rw4;
  unsigned vx, vh, vw, vo, vt, vp, fo, fk;
  u32x2 tag;
  __amdgpu_buffer_rsrc_t rws;
  int j, k0, t0;
};
template <bool IS_A, int I, int E> DI void issue_items(LAS char* lds, LoadCtx& c) {
  if constexpr (I < E) {
    constexpr int K = SchedOf<IS_A>::v.kind[I], N = SchedOf<IS_A>::v.idx[I], U = SchedOf<IS_A>::v.sub[I];
    if constexpr (K == K_H) DMA(c.rx, L_HALO + N * 4096 + c.j * 1024, c.vh, ((2 * N + (c.j >> 1) + c.k0) & 7) * 128, 0);
    if constexpr (K == K_X) DMA(c.rx, x_addr(IS_A, N) + c.j * 4096, c.vx, ((N + c.k0) & 7) * 128 + U * 7168, U * 1024);
    if constexpr (K == K_W) DMA(c.rw, w_addr(IS_A, N, U) - U * 1024 + c.j * 1024, c.vw, ((N + 2 * c.k0) & 15) * 98304, U * 1024);
    if constexpr (K == K_U) {
      constexpr int UA = SchedOf<IS_A>::v.uaddr[N];
      DMA(c.ro, UA + c.j * 1024, c.vo, (((N >> 1) + c.t0) & 15) * 32768 + (N & 1) * (IS_A ? 4096 : 16384), 0);
    }
    if constexpr (K == K_T && N == 0) asm volatile("buffer_load_dword v187, %0, %1, 0 offen" ::"v"(c.vt), "s"(c.ro4) : "v187", "memory");
    if constexpr (K == K_T && N == 1) asm volatile("buffer_load_dword v187, %0, %1, 0 offen" ::"v"(c.vp), "s"(c.rw4) : "v187", "memory");
    issue_items<IS_A, I + 1, E>(lds, c);
  }
}
template <bool IS_A, int S> DI void loader_step(LAS char* lds, LoadCtx& c) {
  if constexpr (has_bar(S)) {
    asm volatile("s_waitcnt vmcnt(%0)" ::"i"(SchedOf<IS_A>::v.wait[S]) : "memory");
    asm volatile("s_barrier" ::: "memory");
    if constexpr (S == 4) {
      asm volatile("" : "+v"(c.tag));
      c.tag = next_tag(c.tag);
    }
    if constexpr ((IS_A && S == 16) || S == S_POST) {
      if (c.j == 3) __builtin_amdgcn_raw_buffer_store_b64(c.tag, c.rws, S == 16 ? c.fo : c.fk, 0, 16);
      SCHED0();
    }
    issue_items<IS_A, SchedOf<IS_A>::v.beg[S + 1], SchedOf<IS_A>::v.beg[S + 2]>(lds, c);
  }
  if constexpr (S < 16) loader_step<IS_A, S + 1>(lds, c);
}
DI void wait_flags(u32x2 tag, u32x4 rs, unsigned voff) {
#pragma nounroll
  for (int spins = 0; spins < SPIN_CAP; ++spins) {
    u32x2 seen;
    asm volatile("buffer_load_dwordx2 %0, %1, %2, 0 offen sc1\n\ts_waitcnt vmcnt(0)" : "=v"(seen) : "v"(voff), "s"(rs) : "memory");
    if (__all(voff == OOB || (seen[0] == tag[0] && seen[1] == tag[1]))) break;
  }
}
DI void loader_init(LoadCtx& c, LAS char* lds, const char* xa, const char* wa, const char* xb, const char* wb, int wave, int lane, int br, int r, int g, int k0, int t0) {
  const int j = wave - 4;
  c.j = j;
  c.k0 = k0;
  c.t0 = t0;
  const int row8 = lane >> 3, ch = lane & 7;
  const unsigned xsw = row8 * 1024 + ((ch ^ (row8 & 6)) * 16);
  c.vx = (r * 128 + j * 32) * 1024 + xsw;
  c.vh = (br ? OOB : (unsigned)((r * 128 - 16 + (j & 1) * 8) * 1024)) + xsw;
  const int fsw = ((row8 >> 1) & 1) | ((j & 1) << 1);
  c.vw = (j * 8 + row8) * 3072 + g * 128 + (((ch >> 1) ^ fsw) * 32) + (ch & 1) * 16;
  asm volatile("" : "+v"(c.vx), "+v"(c.vh), "+v"(c.vw), "+s"(br));
  SCHED0();
  c.rx = mkrsrc(br ? xb : xa, 2048 * 1024);
  c.rw = mkrsrc(br ? wb : wa, 512 * 3072);
  issue_items<true, 0, common_items()>(lds, c);
  SCHED0();
}
constexpr int CH_BEG[5] = {0, 3, 8, 12, 16};
constexpr int CH_ISS[5] = {8, 12, 16, 16, 16};
struct ORelay {
  __amdgpu_buffer_rsrc_t rs;
  unsigned vo, wb[3];
  int rot, j;
};
template <bool IS_A, int T> DI void o_fetch(LAS char* lds, const ORelay& o) {
  __builtin_amdgcn_raw_ptr_buffer_load_lds(o.rs, (LAS void*)(lds + SchedOf<IS_A>::v.oaddr[T] + o.j * 1024), 16, o.vo,
                                           (T >> 2) * 16384 + ((o.rot + (T & 3) * 4096) & 0x3c00), 0, 16);
}
template <bool IS_A, int T, int E> DI void o_fetch_all(LAS char* lds, const ORelay& o) {
  o_fetch<IS_A, T>(lds, o);
  if constexpr (T + 1 < E) o_fetch_all<IS_A, T + 1, E>(lds, o);
}
template <bool IS_A, int T, int E> DI void o_peek(unsigned (&d)[16], const ORelay& o) {
  constexpr int A = SchedOf<IS_A>::v.oaddr[T];
  asm volatile("ds_read_b32 %0, %1 offset:%2" : "=v"(d[T]) : "v"(o.wb[A >> 16]), "i"(A & 65535) : "memory");
  if constexpr (T + 1 < E) o_peek<IS_A, T + 1, E>(d, o);
  else asm volatile("s_waitcnt lgkmcnt(0)" ::: "memory");
  asm volatile("" : "+v"(d[T]));
}
template <int T, int E> DI bool o_missing(const unsigned (&d)[16]) {
  unsigned m = d[T];
#pragma unroll
  for (int i = T + 1; i < E; ++i) m = m > d[i] ? m : d[i];
  return __builtin_amdgcn_ballot_w64(m == SENT) != 0;
}
template <bool IS_A, int T, int E> DI void o_refetch(LAS char* lds, const unsigned (&d)[16], const ORelay& o) {
  if (o_missing<T, T + 1>(d)) o_fetch<IS_A, T>(lds, o);
  if constexpr (T + 1 < E) o_refetch<IS_A, T + 1, E>(lds, d, o);
}
template <bool IS_A, int C> DI void relay_chunk(LAS char* lds, const ORelay& o) {
  constexpr int LO = CH_BEG[C], HI = CH_BEG[C + 1], E = CH_ISS[C];
  unsigned d[16];
  asm volatile("s_waitcnt vmcnt(%0)" ::"i"(E - HI) : "memory");
  o_peek<IS_A, LO, HI>(d, o);
  if (__builtin_expect(o_missing<LO, HI>(d), 0)) {
#pragma nounroll
    for (int spins = 0; spins < SPIN_CAP; ++spins) {
      asm volatile("s_waitcnt vmcnt(0)" ::: "memory");
      o_peek<IS_A, LO, E>(d, o);
      if (!o_missing<LO, HI>(d)) break;
      o_refetch<IS_A, LO, E>(lds, d, o);
    }
    asm volatile("s_waitcnt vmcnt(0)" ::: "memory");
  }
  BARRIER();
  if constexpr (E < CH_ISS[C + 1]) o_fetch_all<IS_A, E, CH_ISS[C + 1]>(lds, o);
  if constexpr (C < 3) relay_chunk<IS_A, C + 1>(lds, o);
}
template <bool IS_A> DI void loader_path(const Params& P, LoadCtx& c, LAS char* lds, const char* po, int lane, int r, int g) {
  const int j = c.j;
  issue_items<IS_A, common_items(), SchedOf<IS_A>::v.beg[1]>(lds, c);
  SCHED0();
  c.ro = mkrsrc(po, 512 * 1024);
  c.ro4 = mkrsrc4(po, 512 * 1024);
  const int row4 = lane >> 4, c16 = lane & 15;
  c.vo = (j * (IS_A ? 8 : 4) + row4) * 1024 + (g & 3) * 256 + (((c16 >> 1) ^ (row4 | ((j & 1) << 2))) * 32) + (c16 & 1) * 16;
  c.vt = lane < 32 ? (P.idx * 128 + j * 32 + lane) * 128 : OOB;
  {
    constexpr int N2 = ((8 - PF_C1) * 192 + 111) / 112;
    const int q = (P.idx >> 3) * 7 + (g > 3 ? g - 1 : g);
    const unsigned i2 = (j * 28 + q) * N2 + lane, l2 = P.k3 * 192 + PF_C1 * 192 + i2;
    c.vp = (lane < N2 && g != 3 && i2 < (8 - PF_C1) * 192) ? __builtin_elementwise_min(l2, l2 - 1536) * 1024 + 384 : OOB;
    c.rw4 = mkrsrc4(P.w, 512 * 3072);
  }
  const u32x4 rws4 = mkrsrc4(P.ws, WS_USED);
  const unsigned line = flag_line(IS_A, r, g, lane);
  asm volatile("buffer_load_dwordx2 %0, %1, %2, 0 offen offset:128 sc1" : "=v"(c.tag) : "v"(line), "s"(rws4) : "memory");
  c.rws = mkrsrc(P.ws, WS_USED);
  c.fo = lane == 0 ? line + g * 8 : OOB;
  c.fk = lane == 32 ? line + (IS_A ? g : r) * 8 : OOB;
  asm volatile("" : "+v"(c.fo), "+v"(c.fk));
  loader_step<IS_A, 0>(lds, c);
  const u32x2 tag = c.tag;
  if constexpr (!IS_A) {
    if (j == 0) wait_flags(tag, rws4, line + (lane & (lane >= 32 ? 15 : 7)) * 8);
    asm volatile("s_waitcnt vmcnt(%0)\n\ts_barrier" ::"i"(SchedOf<IS_A>::v.wait[17]) : "memory");
    constexpr const int* beg = SchedOf<IS_A>::v.beg;
    issue_items<IS_A, beg[18], beg[19]>(lds, c);
    const int l15 = lane & 15, lg = lane >> 4;
    unsigned vk = WS_KV + (g * 4 + j) * 8192 + lane * 16, vz = WS_Z + (g * 4 + j) * 1024 + l15 * 64 + lg * 16;
    unsigned xw = (unsigned)(size_t)lds + SchedOf<IS_A>::v.xch + j * 1024 + lane * 16;
    const int krot = (r & 3) * 2048;
    u32x4 fu, part[8], zp;
#pragma unroll
    for (int i = 0; i < 4; ++i) fu[i] = (l15 >> 1) == (lg & 1) * 4 + i ? ((l15 & 1) ? 0x3F800000u : 0x3F80u) : 0u;
    asm volatile("" : "+v"(fu), "+v"(vk), "+v"(vz), "+v"(xw));
    BARRIER();
    issue_items<IS_A, beg[19], beg[20]>(lds, c);
    BARRIER();
    gather9(part, zp, rws4, vk, vz, krot);
    issue_items<IS_A, beg[20], beg[21]>(lds, c);
    SCHED0();
    poll9<beg[21] - beg[20]>(part, zp, rws4, vk, vz, krot);
    f32x4 kt = {0.f, 0.f, 0.f, 0.f};
    float zf[4];
#pragma unroll
    for (int e = 0; e < 4; ++e) zf[e] = __uint_as_float(zp[e]);
#pragma unroll
    for (int i = 0; i < 8; ++i) {
      kt = MFMA32(__builtin_bit_cast(b16x8, fu), __builtin_bit_cast(b16x8, part[i]), kt);
      SCHED0();
#pragma unroll
      for (int e = (i & 1) * 2; e < (i & 1) * 2 + 2; ++e) {
        if (i >> 1 == 0) zf[e] = dpp_add<0xB1>(zf[e]);
        if (i >> 1 == 1) zf[e] = dpp_add<0x4E>(zf[e]);
        if (i >> 1 == 2) zf[e] = dpp_add<0x141>(zf[e]);
        if (i >> 1 == 3) zf[e] = dpp_add<0x140>(zf[e]);
        asm volatile("" : "+v"(zf[e]));
      }
      asm volatile("" : "+v"(kt));
      SCHED0();
    }
    *(LAS b16x8*)(size_t)xw = cat8(pk4(kt), pk4((f32x4){zf[0], zf[1], zf[2], zf[3]}));
    __builtin_amdgcn_s_waitcnt(0xc07f);
    BARRIER();
    issue_items<IS_A, beg[21], beg[22]>(lds, c);
  }
  if (IS_A && j == 0) wait_flags(tag, rws4, lane < 32 ? line + (lane & 7) * 8 : OOB);
  ORelay o;
  o.rs = c.rws;
  o.vo = WS_O + ((IS_A ? 0 : 16) + r) * 131072 + (g >> 2) * 65536 + lane * 16;
#pragma unroll
  for (int i = 0; i < 3; ++i) {
    o.wb[i] = (unsigned)(size_t)lds + j * 1024 + lane * 16 + i * 65536;
    asm volatile("" : "+v"(o.wb[i]));
  }
  o.rot = (c.t0 + j) * 1024;
  o.j = j;
  SCHED0();
  asm volatile("s_waitcnt vmcnt(0)\n\ts_barrier" ::: "memory");
  if (!IS_A && g == 3) __builtin_amdgcn_s_sleep(O_DELAY3);
  else __builtin_amdgcn_s_sleep(IS_A ? O_DELAY : O_DELAY_B);
  o_fetch_all<IS_A, 0, CH_ISS[0]>(lds, o);
  relay_chunk<IS_A, 0>(lds, o);
  if (j == 0) {
    const bool cl = lane == 0 ? g == 0 : (!IS_A && lane == 32 && r == 0);
    __builtin_amdgcn_raw_buffer_store_b64(tag, mkrsrc(P.ws, WS_USED), cl ? line + 128 : OOB, 0, 16);
  }
  if constexpr (IS_A) asm volatile(".Lcode_end_la:");
  else asm volatile(".Lcode_end_lb:");
  __builtin_amdgcn_endpgm();
}

struct Acc {
  f32x4 q[8], k[9], v[9];
};
struct Frag {
  b16x8 x[9];
  b16x8 w[4];
};
struct Addr {
  unsigned x[2], h[2], w[2];
  unsigned x2[2], h2[2], wq[2][2];
  __amdgpu_buffer_rsrc_t rws;
  u32x4 any;
  unsigned o, kv, z;
};
template <bool IS_A, int S, int SEG> DI b16x8 read_w(const Addr& ad) {
  constexpr int A = w_addr(IS_A, S, SEG) - L_W;
  constexpr int HI = A >= W_HI;
  return cat8(trread(ad.w[HI] + (A - HI * W_HI)), trread(ad.w[HI] + (A - HI * W_HI + 512)));
}
template <int S, int H, int HALF> DI s16x4 read_wq_half(const Addr& ad) {
  constexpr int A = w_addr(false, S, 0) - L_W;
  constexpr int HI = A >= W_HI;
  return trread(ad.wq[H][HI] + (A - HI * W_HI + HALF * 512));
}
template <int S, int H> DI b16x8 read_wq(const Addr& ad) { return cat8(read_wq_half<S, H, 0>(ad), read_wq_half<S, H, 1>(ad)); }
template <bool IS_A, int S, int B> DI b16x8 read_x1(const Addr& ad) {
  constexpr int A = x_addr(IS_A, S >> 1);
  if constexpr (IS_A || B < 4) return ldsread((A >= 65536 ? ad.h[S & 1] : ad.x[S & 1]) + ((A & 65535) + B * 2048));
  else return ldsread((A >= 65536 ? ad.h2[S & 1] : ad.x2[S & 1]) + ((A & 65535) + (B - 4) * 2048));
}
template <bool IS_A, int S, int B = 0> DI void read_x(Frag& f, const Addr& ad) {
  f.x[B + 1] = read_x1<IS_A, S, B>(ad);
  if constexpr (B < 7) read_x<IS_A, S, B + 1>(f, ad);
}
template <bool IS_A, int S> DI void read_frags(Frag& f, const Addr& ad) {
  if constexpr (IS_A) f.w[0] = read_w<IS_A, S, 0>(ad);
  if constexpr (!IS_A && S < QD) {
    f.w[0] = read_wq<S, 0>(ad);
    f.w[3] = read_wq<S, 1>(ad);
  }
  f.w[1] = read_w<IS_A, S, 1>(ad);
  f.w[2] = read_w<IS_A, S, 2>(ad);
  if constexpr (IS_A) f.x[0] = ldsread(ad.h[S & 1] + (S >> 1) * 2048);
  read_x<IS_A, S>(f, ad);
}
template <bool IS_A, int S> DI void mma_step(Acc& a, const Frag& f) {
  if constexpr (IS_A) {
    a.k[0] = MFMA32(f.w[1], f.x[0], a.k[0]);
    a.v[0] = MFMA32(f.x[0], f.w[2], a.v[0]);
  }
#pragma unroll
  for (int b = 0; b < 8; ++b) {
    if constexpr (IS_A) a.q[b] = MFMA32(f.w[0], f.x[b + 1], a.q[b]);
    if constexpr (!IS_A && S < QD) a.q[b] = MFMA32(f.w[(b & 1) * 3], f.x[(b >> 1) + 1], a.q[b]);
    if constexpr (IS_A) a.k[b + 1] = MFMA32(f.w[1], f.x[b + 1], a.k[b + 1]);
    else a.k[b + 1] = MFMA32(f.x[b + 1], f.w[1], a.k[b + 1]);
    a.v[b + 1] = MFMA32(f.x[b + 1], f.w[2], a.v[b + 1]);
  }
}
template <bool IS_A, int S> DI void gemm1_step(Acc& a, Frag& f0, Frag& f1, Addr& ad) {
  SCHED0();
  if constexpr ((IS_A && S == 16) || S == S_POST) __builtin_amdgcn_s_waitcnt(0x0070);
  else __builtin_amdgcn_s_waitcnt(0xc07f);
  if constexpr (has_bar(S)) asm volatile("s_barrier" ::: "memory");
  SCHED0();
  if constexpr (S <= 15) {
    constexpr int NST = IS_A ? clr_cnt(true, S) + clr_cnt(false, S) : S == S_CLRK ? 2 : 0;
    read_frags<IS_A, S>((S & 1) ? f1 : f0, ad);
    if constexpr (NST > 0) {
      u32x4 sv = ad.any;
      sv[0] = SENT;
      if constexpr (!IS_A && S == S_CLRK) {
        store_sc1(sv, ad.rws, ad.kv);
        store_sc1(sv, ad.rws, ad.z);
      }
      if constexpr (IS_A) {
#pragma unroll
        for (int i = clr_before(false, S); i < clr_before(false, S + 1); ++i) __builtin_amdgcn_raw_buffer_store_b128(sv, ad.rws, ad.kv, i * 16384, 16);
#pragma unroll
        for (int i = clr_before(true, S); i < clr_before(true, S + 1); ++i) __builtin_amdgcn_raw_buffer_store_b128(sv, ad.rws, ad.o, i * 32768, 16);
      }
    }
    if constexpr (S >= 1) {
      mma_step<IS_A, S - 1>(a, (S & 1) ? f0 : f1);
      constexpr int NR = IS_A ? 15 : S < QD ? 16 : 12;
#pragma unroll
      for (int i = 0; i < NR; ++i) {
        SGB(0x8, 1);
        SGB(0x100, 1);
      }
      if constexpr (NST > 0) {
#pragma unroll
        for (int i = 0; i < NST; ++i) {
          SGB(0x8, 1);
          SGB(0x2, 2);
          SGB(0x40, 1);
        }
      }
      SGB(0x8, 16);
    }
    gemm1_step<IS_A, S + 1>(a, f0, f1, ad);
  }
}
struct PhiSeq {
  float t[8], m[8];
  unsigned c[4];
};
constexpr int PHI_KIND[36] = {0, 0, 0, 0, 0, 1, 2, 1, 1, 2, 1, 1, 2, 0, 0, 0, 2, 2, 1, 1, 2, 1, 3, 3, 3, 3, 3, 2, 2, 4, 4, 3, 3, 3, 4, 4};
constexpr int PHI_ELEM[36] = {0, 1, 2, 3, 4, 0, 0, 1, 2, 1, 3, 4, 2, 5, 6, 7, 3, 4, 5, 6, 5, 7, 0, 1, 2, 3, 4, 6, 7, 0, 1, 5, 6, 7, 2, 3};
template <int K> DI void phi_op(PhiSeq& p, const f32x4& in0, const f32x4& in1) {
  constexpr int KD = PHI_KIND[K], E = PHI_ELEM[K];
  const float u = E < 4 ? in0[E & 3] : in1[E & 3];
  if constexpr (KD == 0) p.t[E] = __builtin_fmaf(u, LOG2E, -EPS * LOG2E);
  if constexpr (KD == 1) p.t[E] = __builtin_amdgcn_fmed3f(__builtin_amdgcn_exp2f(p.t[E]), 0.f, 1.f);
  if constexpr (KD == 2) p.m[E] = fmx(u, EPS);
  if constexpr (KD == 3) p.t[E] += p.m[E];
  if constexpr (KD == 4) p.c[E] = pk2(p.t[2 * E], p.t[2 * E + 1]);
  if constexpr (KD == 2) asm volatile("" : "+v"(p.m[E]));
  else if constexpr (KD == 4) asm volatile("" : "+v"(p.c[E]));
  else asm volatile("" : "+v"(p.t[E]));
  SCHED0();
}
template <int K0, int K1> DI void phi_ops(PhiSeq& p, const f32x4& in0, const f32x4& in1) {
  if constexpr (K0 < K1) {
    phi_op<K0>(p, in0, in1);
    phi_ops<K0 + 1, K1>(p, in0, in1);
  }
}
constexpr int phi_fit(int n) {
  int k = 0;
  while (k < 36 && (n -= PHI_KIND[k] == 1 ? 2 : 1) >= 0) ++k;
  return k;
}
struct KState {
  PhiSeq ph[4];
  unsigned vc[4][4];
  f32x4 kv, zz;
};
template <int E> DI void k_mfma(Acc& a, const Frag& f) {
  if constexpr (E < 8) a.k[E + 1] = MFMA32(f.x[E + 1], f.w[1], a.k[E + 1]);
  else a.v[E - 7] = MFMA32(f.x[E - 7], f.w[2], a.v[E - 7]);
  SCHED0();
}
template <int I, bool Z> DI void kv_mfma(KState& k, b16x8 ones) {
  const b16x8 pkf = __builtin_bit_cast(b16x8, (u32x4){k.ph[I].c[0], k.ph[I].c[1], k.ph[I].c[2], k.ph[I].c[3]});
  if constexpr (Z) k.zz = MFMA32(pkf, ones, k.zz);
  else k.kv = MFMA32(pkf, __builtin_bit_cast(b16x8, (u32x4){k.vc[I][0], k.vc[I][1], k.vc[I][2], k.vc[I][3]}), k.kv);
  SCHED0();
}
template <int P> DI void k_ops(Acc& a, const Frag& f, KState& k, b16x8 ones) {
  constexpr int I = P / 40, R = P % 40;
  if constexpr (P < 36 && P % 3 == 0) k_mfma<4 + P / 3>(a, f);
  if constexpr (I > 0 && R == 3) kv_mfma<I - 1, false>(k, ones);
  if constexpr (I > 0 && R == 6) kv_mfma<I - 1, true>(k, ones);
  constexpr int C = I == 0 ? R - 36 : R < 4 ? R : -1;
  if constexpr (C >= 0) {
    const f32x4& v = a.v[2 * I + 1 + (C >> 1)];
    k.vc[I][C] = pk2(v[(C & 1) * 2], v[(C & 1) * 2 + 1]);
    asm volatile("" : "+v"(k.vc[I][C]));
    SCHED0();
  } else {
    phi_op<I == 0 ? R : R - 4>(k.ph[I], a.k[2 * I + 1], a.k[2 * I + 2]);
  }
  if constexpr (P < 159) k_ops<P + 1>(a, f, k, ones);
}
constexpr int QLA = 8;
struct QState {
  s16x4 wq[14][2][2];
  b16x8 xf[40];
  PhiSeq ph[4];
};
template <int I> DI void wq_read1(QState& q, const Addr& ad) {
  if constexpr (I >= 0 && I < 4 * (14 - QD)) q.wq[QD + I / 4][(I >> 1) & 1][I & 1] = read_wq_half<QD + I / 4, (I >> 1) & 1, I & 1>(ad);
}
constexpr int phi_slots(int m) { return m < 2 ? 0 : (m - 1) * 5 / 2 + ((m - 1) & 1); }
static_assert(phi_fit(phi_slots(19)) == 36, "phi");
template <int M, int END> DI void q_head(Acc& a, const Frag& f0, const Frag& f1, QState& q, const Addr& ad) {
  const Frag& f = M < 8 ? f0 : f1;
  MFMA32_IP(f.w[(M & 1) * 3], f.x[((M & 7) >> 1) + 1], a.q[M & 7]);
  SCHED0();
  if constexpr (M < 2 * (QG - QD)) {
    wq_read1<2 * M>(q, ad);
    wq_read1<2 * M + 1>(q, ad);
  }
  if constexpr (M >= 16 - QLA) q.xf[M - (16 - QLA)] = read_x1<false, QD + M - (16 - QLA), 0>(ad);
  SCHED0();
  if constexpr (M + 1 < END) q_head<M + 1, END>(a, f0, f1, q, ad);
}
template <int M> DI void q_body(Acc& a, QState& q, const Addr& ad) {
  constexpr int N = M >> 1, B = N / 10, S = QD + N % 10, H = M & 1;
  if constexpr (M == 0) __builtin_amdgcn_s_waitcnt(0xc07f | (QLA << 8));
  if constexpr (M == 0 || M == 20 || M == 40) BARRIER();
  if constexpr (M >= 20 && M % 10 == 0) {
    constexpr int OUT = (40 - N < QLA ? 40 - N : QLA) - 5;
    __builtin_amdgcn_s_waitcnt(0xc07f | (OUT << 8));
    SCHED0();
  }
  if constexpr (S < 13) MFMA32_IP(cat8(q.wq[S][H][0], q.wq[S][H][1]), q.xf[N], a.q[2 * B + H]);
  else a.q[2 * B + H] = MFMA32(cat8(q.wq[S][H][0], q.wq[S][H][1]), q.xf[N], a.q[2 * B + H]);
  SCHED0();
  if constexpr (M < 12) {
    wq_read1<4 * (QG - QD) + 2 * M>(q, ad);
    wq_read1<4 * (QG - QD) + 2 * M + 1>(q, ad);
  }
  if constexpr (H == 1 && N + QLA < 40) q.xf[N + QLA] = read_x1<false, QD + (N + QLA) % 10, (N + QLA) / 10>(ad);
  SCHED0();
  if constexpr (B > 0) phi_ops<phi_fit(phi_slots(M % 20 - 1)), phi_fit(phi_slots(M % 20))>(q.ph[B - 1], a.q[2 * B - 2], a.q[2 * B - 1]);
  if constexpr (M < 79) q_body<M + 1>(a, q, ad);
}
static_assert(QD == 4 && QG == 8, "q phase");

template <bool IS_A, int T> DI void wo_read(b16x8 (&wf)[16][2], const unsigned (&ub)[3][2]) {
  constexpr int A0 = SchedOf<IS_A>::v.uaddr[2 * T], A1 = SchedOf<IS_A>::v.uaddr[2 * T + 1];
#pragma unroll
  for (int j = 0; j < 2; ++j) wf[T][j] = cat8(trread(ub[A0 >> 16][j] + (A0 & 65535)), trread(ub[A1 >> 16][j] + (A1 & 65535)));
  if constexpr (T < 15) wo_read<IS_A, T + 1>(wf, ub);
}
template <bool IS_A, int R> DI void o_read1(b16x8 (&of)[64], const unsigned (&ob)[3]) {
  if constexpr (R < 64) {
    constexpr int A = SchedOf<IS_A>::v.oaddr[R >> 2];
    of[R] = ldsread(ob[A >> 16] + ((A & 65535) + (R & 3) * 1024));
    SCHED0();
  }
}
struct OutSeq {
  u32x2 lo, hi;
  u32x4 gr;
};
template <int B, int P> DI void out_op(OutSeq& q, const f32x4 (&c2)[4][2], __amdgpu_buffer_rsrc_t rout, unsigned oo) {
  if constexpr (P == 0) {
    q.lo = __builtin_bit_cast(u32x2, pk4(c2[B][0]));
    asm volatile("" : "+v"(q.lo));
  }
  if constexpr (P == 1) {
    q.hi = __builtin_bit_cast(u32x2, pk4(c2[B][1]));
    asm volatile("" : "+v"(q.hi));
  }
  if constexpr (P == 2) {
    q.gr = pair_granule(__builtin_bit_cast(s16x4, q.lo), __builtin_bit_cast(s16x4, q.hi));
    asm volatile("" : "+v"(q.gr));
  }
  if constexpr (P == 3) __builtin_amdgcn_raw_buffer_store_b128(q.gr, rout, oo, B * 32768, 16);
  SCHED0();
}
template <bool IS_A, int M>
DI void gemm2_step(f32x4 (&c2)[4][2], const b16x8 (&wf)[16][2], b16x8 (&of)[64], const unsigned (&ob)[3], OutSeq& q,
                   __amdgpu_buffer_rsrc_t rout, unsigned oo) {
  constexpr int B = M >> 5, I = M & 31, T = I >> 1, J = I & 1, R = (M >> 1) + OLA;
  if constexpr (J == 0 && (R == 12 || R == 32 || R == 48)) BARRIER();
  if constexpr (T == 0) asm volatile("v_mfma_f32_16x16x32_bf16 %0, %1, %2, 0" : "=&v"(c2[B][J]) : "v"(wf[T][J]), "v"(of[M >> 1]));
  else if constexpr (T < 15) MFMA32_IP(wf[T][J], of[M >> 1], c2[B][J]);
  else c2[B][J] = MFMA32(wf[T][J], of[M >> 1], c2[B][J]);
  SCHED0();
  if constexpr (J == 0) o_read1<IS_A, R>(of, ob);
  if constexpr (B > 0 && J == 1 && T >= 1 && T < 5) out_op<B - 1, T - 1>(q, c2, rout, oo);
  if constexpr (M < 127) gemm2_step<IS_A, M + 1>(c2, wf, of, ob, q, rout, oo);
}
template <bool IS_A, int R> DI void o_read_head(b16x8 (&of)[64], const unsigned (&ob)[3]) {
  o_read1<IS_A, R>(of, ob);
  if constexpr (R + 1 < OLA) o_read_head<IS_A, R + 1>(of, ob);
}

template <bool IS_A>
DI void compute_path(const Params& P, LAS char* lds, int wave, int lane, int r, int g) {
  constexpr int BR = IS_A ? 0 : 1;
  lane = fresh_lane();
  int l15 = lane & 15, lg = lane >> 4;
  const __amdgpu_buffer_rsrc_t rws = mkrsrc(P.ws, WS_USED);
  const unsigned lbase = (unsigned)(size_t)lds;

  Acc a;
  const f32x4 zero = {0.f, 0.f, 0.f, 0.f};
  const f32x4 eps = {EPS, EPS, EPS, EPS};
#pragma unroll
  for (int b = 0; b < 8; ++b) a.q[b] = IS_A ? zero : eps;
#pragma unroll
  for (int b = 0; b < 9; ++b) { a.k[b] = IS_A ? zero : eps; a.v[b] = zero; }
  Frag f0, f1;
  Addr ad;
  {
    ad.o = off_o(BR, r, g, wave, lane);
    asm volatile("" : "+v"(ad.o));
    if constexpr (IS_A) {
      ad.kv = off_o(1, r, g, wave, lane);
      asm volatile("" : "+v"(ad.kv));
    } else {
      ad.kv = off_kv(r, g, wave, lane);
      ad.z = off_z(r, g, wave, lane);
      asm volatile("" : "+v"(ad.kv), "+v"(ad.z));
    }
    ad.any = __builtin_nondeterministic_value(ad.any);
    ad.rws = rws;
    const unsigned xb = lbase + l15 * 128 + ((lg ^ (l15 & 6)) * 16);
    const unsigned own = IS_A ? 0 : (wave >> 1) * 8192;
    ad.x[0] = xb + own;
    ad.x[1] = (xb ^ 64) + own;
    ad.h[0] = (IS_A ? L_HALO : 65536) + ad.x[0];
    ad.h[1] = (IS_A ? L_HALO : 65536) + ad.x[1];
    const int q = l15 >> 2, p = l15 & 3;
    const int fsw = ((q >> 1) & 1) | ((lg & 1) << 1);
    ad.w[0] = lbase + L_W + lg * 1024 + q * 128 + ((wave ^ fsw) * 32) + p * 8;
    ad.w[1] = ad.w[0] + W_HI;
    asm volatile("" : "+v"(ad.w[0]), "+v"(ad.w[1]), "+v"(ad.h[0]), "+v"(ad.h[1]));
    if constexpr (!IS_A) {
#pragma unroll
      for (int i = 0; i < 2; ++i) {
        ad.x2[i] = ad.x[i] + 8192 - 2 * own;
        ad.h2[i] = ad.x2[i] + 65536;
        ad.wq[i][0] = lbase + L_W + lg * 1024 + q * 128 + ((((wave & 1) * 2 + i) ^ fsw) * 32) + p * 8;
        ad.wq[i][1] = ad.wq[i][0] + W_HI;
        asm volatile("" : "+v"(ad.x2[i]), "+v"(ad.h2[i]), "+v"(ad.wq[i][0]), "+v"(ad.wq[i][1]));
      }
    }
  }
  unsigned xr = lbase + SchedOf<false>::v.xch + lane * 16 + (wave & 1) * 2048;
  if constexpr (!IS_A) asm volatile("" : "+v"(xr));
  {
    constexpr int N1 = (PF_C1 * 192 + 27) / 28;
    const int q = (P.idx >> 3) * 7 + (g > 3 ? g - 1 : g);
    const unsigned l1 = P.k3 * 192 + q * N1 + lane;
    if (wave == 0 && g != 3)
      __builtin_amdgcn_raw_ptr_buffer_load_lds(mkrsrc(P.w, 512 * 3072), (LAS void*)(lds + L_SP + 2048), 4,
                                               lane < N1 ? __builtin_elementwise_min(l1, l1 - 1536) * 1024 + 384 : OOB, 0, 0, 0);
    if (wave == 1)
      __builtin_amdgcn_raw_ptr_buffer_load_lds(mkrsrc(P.x, 2048 * 1024), (LAS void*)(lds + L_SP + 2304), 4,
                                               lane < 16 ? (r * 128 + g * 16 + lane) * 1024 + 384 : OOB, 0, 0, 0);
  }
  gemm1_step<IS_A, 0>(a, f0, f1, ad);
  asm volatile("" : "+v"(ad.o));
  lane = fresh_lane();
  l15 = lane & 15;
  lg = lane >> 4;
  const s16x8 ones_s = {0x3F80, 0x3F80, 0x3F80, 0x3F80, 0x3F80, 0x3F80, 0x3F80, 0x3F80};
  const b16x8 ones = __builtin_bit_cast(b16x8, ones_s);

  if constexpr (IS_A) {
    s16x4 kf[9], vf[9], qf[8];
#pragma unroll
    for (int b = 0; b < 9; ++b) a.k[b] = MFMA32(f1.w[1], f1.x[b], a.k[b]);
#pragma unroll
    for (int b = 0; b < 8; ++b) a.q[b] = MFMA32(f1.w[0], f1.x[b + 1], a.q[b]);
#pragma unroll
    for (int b = 0; b < 9; ++b) a.v[b] = MFMA32(f1.x[b], f1.w[2], a.v[b]);
    const float NEG = -__builtin_inff();
    f32x4 cprev, ccur;
    unsigned sel[4];
#pragma unroll
    for (int i = 0; i < 4; ++i) {
      const int c = 4 * lg + i;
      const bool par = ((c ^ l15) & 1) == 0;
      cprev[i] = (par && c > l15) ? 0.f : NEG;
      ccur[i] = (par && c <= l15) ? 0.f : NEG;
    }
    {
      const unsigned s0 = (l15 & 1) ? 0x01000c0cu : 0x0c0c0100u, s1 = (l15 & 1) ? 0x03020c0cu : 0x0c0c0302u, sz = 0x0c0c0c0cu;
      const int cA = 4 * lg + (l15 & 1), cB = cA + 2;
      sel[0] = cA > l15 ? s0 : sz;
      sel[1] = cB > l15 ? s1 : sz;
      sel[2] = cA > l15 ? sz : s0;
      sel[3] = cB > l15 ? sz : s1;
    }
    f32x4 cfirst = cprev;
    if (r == 0) cfirst = (f32x4){NEG, NEG, NEG, NEG};
    const unsigned oofs = ad.o;
#pragma unroll
    for (int b = 0; b < 9; ++b) kf[b] = pk4(a.k[b]);
#pragma unroll
    for (int b = 0; b < 8; ++b) qf[b] = pk4(a.q[b]);
#pragma unroll
    for (int i = 0; i < 26; ++i) {
      SGB(0x8, 1);
      SGB(0x2, 2);
    }
    SCHED0();
    f32x4 s0[8], s1[8];
#pragma unroll
    for (int b = 0; b < 8; ++b) {
      s0[b] = MFMA16(kf[b], qf[b], b == 0 ? cfirst : cprev);
      s1[b] = MFMA16(kf[b + 1], qf[b], ccur);
    }
#pragma unroll
    for (int b = 0; b < 9; ++b) vf[b] = pk4(a.v[b]);
#pragma unroll
    for (int i = 0; i < 16; ++i) {
      SGB(0x8, 1);
      SGB(0x2, 1);
    }
    SCHED0();
    const float C1 = 0.25f * LOG2E;
    float t0v[8], t1v[8], mc[8];
#pragma unroll
    for (int b = 0; b < 8; ++b) {
      t0v[b] = fmx(fmx(s0[b][0], s0[b][1]), fmx(s1[b][0], s1[b][1]));
      t1v[b] = fmx(fmx(s0[b][2], s0[b][3]), fmx(s1[b][2], s1[b][3]));
      mc[b] = fmx(t0v[b], t1v[b]);
    }
#pragma unroll
    for (int b = 0; b < 8; b += 4) {
      const f32x2 ab = swap16(mc[b], mc[b + 1]), cd = swap16(mc[b + 2], mc[b + 3]);
      const f32x2 w2 = swap32(fmx(ab[0], ab[1]), fmx(cd[0], cd[1]));
      const float w = fmx(w2[0], w2[1]) * C1;
      const f32x2 h2 = swap32(w, w);
      const f32x2 m01 = swap16(h2[0], h2[0]), m23 = swap16(h2[1], h2[1]);
      mc[b] = m01[0]; mc[b + 1] = m01[1]; mc[b + 2] = m23[0]; mc[b + 3] = m23[1];
    }
    SCHED0();
    f32x4 o[8], sm[8];
#pragma unroll
    for (int b = 0; b < 8; ++b) {
      const float e0 = __builtin_amdgcn_exp2f(__builtin_fmaf(t0v[b], C1, -mc[b]));
      const float e1 = __builtin_amdgcn_exp2f(__builtin_fmaf(t1v[b], C1, -mc[b]));
      const unsigned p = pk2(e0, e1);
      const u32x4 pu = {__builtin_amdgcn_perm(p, p, sel[0]), __builtin_amdgcn_perm(p, p, sel[1]), __builtin_amdgcn_perm(p, p, sel[2]),
                        __builtin_amdgcn_perm(p, p, sel[3])};
      const b16x8 pf = __builtin_bit_cast(b16x8, pu);
      sm[b] = MFMA32(ones, pf, zero);
      o[b] = MFMA32(cat8(vf[b], vf[b + 1]), pf, zero);
    }
    SCHED0();
    s16x4 ob[8];
#pragma unroll
    for (int b = 0; b < 8; ++b) {
      ob[b] = pk4(o[b] * __builtin_amdgcn_rcpf(sm[b][0]));
      if (b & 1) store_sc1(no_sent(pair_granule(ob[b - 1], ob[b])), rws, oofs + (b >> 1) * 32768);
    }
  } else {
    KState ks;
    QState qs;
    ks.kv = zero;
    ks.zz = zero;
    SCHED0();
    k_mfma<0>(a, f1);
    f0.w[0] = read_wq<14, 0>(ad);
    SCHED0();
    k_mfma<1>(a, f1);
    f0.w[3] = read_wq<14, 1>(ad);
    SCHED0();
    k_mfma<2>(a, f1);
    f1.w[0] = read_wq<15, 0>(ad);
    SCHED0();
    k_mfma<3>(a, f1);
    f1.w[3] = read_wq<15, 1>(ad);
    SCHED0();
    k_ops<0>(a, f1, ks, ones);
    kv_mfma<3, false>(ks, ones);
    kv_mfma<3, true>(ks, ones);
    q_head<0, KTAIL>(a, f0, f1, qs, ad);
    const s16x4 kvp = pk4(ks.kv);
    store_sc1(no_sent(pair_granule(kvp, kvp)), rws, ad.kv);
    store_sc1(no_sent(__builtin_bit_cast(u32x4, ks.zz)), rws, ad.z);
    SCHED0();
    q_head<KTAIL, 16>(a, f0, f1, qs, ad);
    q_body<0>(a, qs, ad);
    phi_ops<0, 36>(qs.ph[3], a.q[6], a.q[7]);
    s16x4 pq[8];
#pragma unroll
    for (int b = 0; b < 8; ++b) pq[b] = __builtin_bit_cast(s16x4, (u32x2){qs.ph[b >> 1].c[(b & 1) * 2], qs.ph[b >> 1].c[(b & 1) * 2 + 1]});
    const unsigned oofs = ad.o;
    f32x4 epsv = eps;
    asm volatile("" : "+v"(epsv));
    BARRIER();
    s16x8 kz[2];
#pragma unroll
    for (int h = 0; h < 2; ++h) kz[h] = __builtin_bit_cast(s16x8, ldsread(xr + h * 1024));
    f32x4 num[8], den[8];
    SCHED0();
#pragma unroll
    for (int b = 0; b < 8; ++b) {
      den[b] = MFMA16(__builtin_shufflevector(kz[b & 1], kz[b & 1], 4, 5, 6, 7), pq[b], epsv);
      num[b] = MFMA16(__builtin_shufflevector(kz[b & 1], kz[b & 1], 0, 1, 2, 3), pq[b], zero);
    }
    SCHED0();
    s16x4 ob[8];
#pragma unroll
    for (int b = 0; b < 8; ++b) {
      ob[b] = pk4(num[b] * __builtin_amdgcn_rcpf(den[b][0]));
      if (b & 1) store_sc1(no_sent(__builtin_bit_cast(u32x4, cat8(ob[b - 1], ob[b]))), rws, oofs + (b >> 1) * 16384);
    }
  }
  BARRIER();
  unsigned ub[3][2], ob[3];
  {
    const int q = l15 >> 2, p = l15 & 3;
    const int sw = q | ((lg & 1) << 2);
    const unsigned u0 = lbase + lg * 1024 + q * 256 + p * 8;
#pragma unroll
    for (int w = 0; w < 3; ++w) {
#pragma unroll
      for (int j = 0; j < 2; ++j) {
        ub[w][j] = u0 + (((2 * wave + j) ^ sw) * 32) + w * 65536;
        asm volatile("" : "+v"(ub[w][j]));
      }
      ob[w] = lbase + lane * 16 + w * 65536;
      asm volatile("" : "+v"(ob[w]));
    }
  }
  b16x8 wf[16][2], of[64];
  wo_read<IS_A, 0>(wf, ub);
  f32x4 c2[4][2];
  const __amdgpu_buffer_rsrc_t rout = mkrsrc(P.out, 2048 * 2048);
  unsigned oo = (r * 128 + (g >> 2) * 64 + l15) * 2048 + BR * 1024 + (g & 3) * 256 + wave * 64 + ((lg & 1) * 32 + (lg >> 1) * 16);
  asm volatile("" : "+v"(oo));
  SCHED0();
  __builtin_amdgcn_s_waitcnt(0xc07f);
  BARRIER();
  o_read_head<IS_A, 0>(of, ob);
  OutSeq oq;
  gemm2_step<IS_A, 0>(c2, wf, of, ob, oq, rout, oo);
  __builtin_amdgcn_raw_buffer_store_b128(pair_granule(pk4(c2[3][0]), pk4(c2[3][1])), rout, oo, 3 * 32768, 16);
  if constexpr (IS_A) asm volatile(".Lcode_end_a:");
  else asm volatile(".Lcode_end_b:");
  __builtin_amdgcn_endpgm();
}

__global__ __launch_bounds__(512, 2) void fused_kernel(const char* xa, const char* wa, const char* xb, const char* wb, char* ws,
                                                       const char* oa, const char* ob, char* out) {
  extern __shared__ __attribute__((aligned(16))) char lds_raw[];
  LAS char* lds = (LAS char*)lds_raw;
  const int wave = __builtin_amdgcn_readfirstlane(threadIdx.x >> 6);
  const int lane = threadIdx.x & 63;
  const int bid = blockIdx.x;
  const int xcd = bid & 7, idx = bid >> 3;
  const int br = xcd & 1;
  const int r = (xcd >> 1) * 4 + (idx >> 3), g = idx & 7;
  const char* po = br ? ob : oa;
  Params P;
  P.ws = ws;
  P.out = out;
  P.w = br ? wb : wa;
  P.idx = idx;
  const int xq = r >> 2;
  const int k0 = ((g + xq) & 3) * 2;
  P.k3 = ((3 + xq) & 3) * 2;
  P.x = br ? xb : xa;
  const int t0 = br ? 8 : (((g >> 2) + 2 * r) * 2 + (g & 3) * 4) & 15;
  {
    u64 pc;
    int len;
    asm volatile("s_mov_b32 m0, %2\n\ts_getpc_b64 %0\n.Lcode_ref:\n\ts_max_i32 %1, .Lcode_end_a-.Lcode_ref, 0\n\t"
                 "s_max_i32 %1, .Lcode_end_b-.Lcode_ref, %1\n\ts_max_i32 %1, .Lcode_end_la-.Lcode_ref, %1\n\t"
                 "s_max_i32 %1, .Lcode_end_lb-.Lcode_ref, %1"
                 : "=s"(pc), "=s"(len) : "s"((unsigned)(size_t)(lds + L_SP) + wave * 256) : "scc", "memory");
    asm volatile("buffer_load_dword %0, %1, 0 offen lds" ::"v"(idx * 2048 + wave * 256 + (lane & 1) * 128), "s"(mkrsrc4((const void*)pc, len))
                 : "memory");
  }
  if (wave < 4) {
    if (br == 0) compute_path<true>(P, lds, wave, lane, r, g);
    else compute_path<false>(P, lds, wave, lane, r, g);
  } else {
    LoadCtx c;
    loader_init(c, lds, xa, wa, xb, wb, wave, lane, br, r, g, k0, t0);
    if (br == 0) loader_path<true>(P, c, lds, po, lane, r, g);
    else loader_path<false>(P, c, lds, po, lane, r, g);
  }
}

extern "C" void kernel_launch(void* const* d_in, const int* in_sizes, int n_in, void* d_out, int out_size, void* d_ws,
                              size_t ws_size, hipStream_t stream) {
  (void)hipFuncSetAttribute((const void*)fused_kernel, hipFuncAttributeMaxDynamicSharedMemorySize, LDS_BYTES);
  hipLaunchKernelGGL(fused_kernel, dim3(256), dim3(512), LDS_BYTES, stream, (const char*)d_in[0], (const char*)d_in[1],
                     (const char*)d_in[3], (const char*)d_in[4], (char*)d_ws, (const char*)d_in[2], (const char*)d_in[5], (char*)d_out);
}
```
